```python
import torch

def _softmax(x, axis=-1):
    return torch.softmax(x, dim=axis)

def _gelu(x):
    return torch.nn.functional.gelu(x, approximate='tanh')

def _silu(x):
    return torch.nn.functional.silu(x)

def _sigmoid(x):
    return torch.sigmoid(x)

def _layernorm(x, axis=-1, eps=1e-05):
    m = x.mean(dim=axis, keepdim=True)
    v = x.var(dim=axis, keepdim=True, unbiased=False)
    return (x - m) / torch.sqrt(v + eps)

def _scatter_add(tgt, idx, src, axis=0):
    return tgt.clone().index_add_(axis, idx, src)

def _segment_sum(src, seg, n):
    out = torch.zeros((n,) + src.shape[1:], dtype=src.dtype)
    return out.index_add_(0, seg, src)

def _pad(x, pw):
    flat = tuple((v for pair in reversed(pw) for v in pair))
    return torch.nn.functional.pad(x, flat)
kernel_name = 'parallel_sum__memory_aug_recurrent_mem_tr__glu_gated_bilinear__prompt'

def setup_inputs():

    _w = lambda a: (lambda t: t.to(torch.bfloat16) if t.is_floating_point() else t)(torch.as_tensor(a))

    def _A_setup():
        B, S, Dm = (1, 2048, 448)
        H, Dh = (16, 256)
        M, seg = (8, 32)
        seg = min(seg, S)
        S = S // seg * seg
        return {'x': _w((lambda *s: torch.randn(*s, dtype=torch.bfloat16) * 0.05)(B, S, Dm)), 'mem0': _w((lambda *s: torch.randn(*s, dtype=torch.bfloat16) * 0.2)(B, M, Dm)), 'Wq': _w((lambda *s: torch.randn(*s, dtype=torch.bfloat16) * 1.6)(Dm, H, Dh) * Dm ** (-0.5)), 'Wk': _w((lambda *s: torch.randn(*s, dtype=torch.bfloat16) * 1.6)(Dm, H, Dh) * Dm ** (-0.5)), 'Wv': _w((lambda *s: torch.randn(*s, dtype=torch.bfloat16) * 0.2)(Dm, H, Dh) * Dm ** (-0.5)), 'Wo': _w((lambda *s: torch.randn(*s, dtype=torch.bfloat16) * 0.2)(H, Dh, Dm) * (H * Dh) ** (-0.5)), 'seg_len': seg}
    _A_in = _A_setup()

    def _B_setup():
        B, S, D, F = (1, 2048, 448, 1792)
        return {'x': _w((lambda *s: torch.randn(*s, dtype=torch.bfloat16) * 0.05)(B, S, D)), 'w_gate': _w((lambda *s: torch.randn(*s, dtype=torch.bfloat16) * 0.2)(D, F) * D ** (-0.5)), 'w_up': _w((lambda *s: torch.randn(*s, dtype=torch.bfloat16) * 0.2)(D, F) * D ** (-0.5)), 'w_down': _w((lambda *s: torch.randn(*s, dtype=torch.bfloat16) * 0.2)(F, D) * F ** (-0.5))}
    _B_in = _B_setup()
    return {'A_x': _A_in['x'], 'A_mem0': _A_in['mem0'], 'A_Wq': _A_in['Wq'], 'A_Wk': _A_in['Wk'], 'A_Wv': _A_in['Wv'], 'A_Wo': _A_in['Wo'], 'A_seg_len': _A_in['seg_len'], 'B_x': _B_in['x'], 'B_w_gate': _B_in['w_gate'], 'B_w_up': _B_in['w_up'], 'B_w_down': _B_in['w_down']}

def reference(A_x, A_mem0, A_Wq, A_Wk, A_Wv, A_Wo, A_seg_len, B_x, B_w_gate, B_w_up, B_w_down):

    def _A_ref(x, mem0, Wq, Wk, Wv, Wo, seg_len):
        B, S, Dm = x.shape
        H, Dh = (Wq.shape[1], Wq.shape[2])
        M = mem0.shape[1]
        scale = Dh ** (-0.5)
        n_seg = S // seg_len
        L = M + seg_len
        idx = torch.arange(L)
        mask = (idx[:, None] < M) | (idx[:, None] >= idx[None, :])
        mem = mem0
        outs = []
        for s in range(n_seg):
            xs = x[:, s * seg_len:(s + 1) * seg_len, :].clone()
            h = torch.concatenate([mem, xs], axis=1)
            q = torch.einsum('btd,dhe->bhte', h, Wq)
            k = torch.einsum('btd,dhe->bhte', h, Wk)
            v = torch.einsum('btd,dhe->bhte', h, Wv)
            scores = torch.einsum('bhqe,bhke->bhqk', q, k) * scale
            scores = torch.where(mask[None, None], scores, -1000000000.0)
            p = _softmax(scores, axis=-1)
            o = torch.einsum('bhqk,bhke->bhqe', p, v)
            o = torch.einsum('bhte,hed->btd', o, Wo)
            mem = o[:, :M, :].clone()
            outs.append(o[:, M:, :])
        return torch.concatenate(outs, axis=1)

    def _B_ref(x, w_gate, w_up, w_down):
        g = torch.einsum('bsd,df->bsf', x, w_gate)
        u = torch.einsum('bsd,df->bsf', x, w_up)
        h = g * u
        return torch.einsum('bsf,fd->bsd', h, w_down)
    x = A_x
    xn = _layernorm(x, axis=-1)
    return x + _A_ref(xn, A_mem0, A_Wq, A_Wk, A_Wv, A_Wo, A_seg_len) + _B_ref(xn, B_w_gate, B_w_up, B_w_down)
```

```cpp
#include <hip/hip_runtime.h>
#include <hip/hip_bf16.h>
#include <stdint.h>

typedef __bf16 bf16x8 __attribute__((ext_vector_type(8)));
typedef __bf16 bf16x2 __attribute__((ext_vector_type(2)));
typedef short s16x4 __attribute__((ext_vector_type(4)));
typedef float f32x2 __attribute__((ext_vector_type(2)));
typedef float f32x4 __attribute__((ext_vector_type(4)));
typedef float f32x16 __attribute__((ext_vector_type(16)));
typedef unsigned u32x2 __attribute__((ext_vector_type(2)));
typedef unsigned u32x4 __attribute__((ext_vector_type(4)));
typedef unsigned short u16;
#define LAS __attribute__((address_space(3)))
#define GAS __attribute__((address_space(1)))
#define DEV __device__ __forceinline__

constexpr int DM = 448, NH = 16, NSEG = 64, FF = 1792;
constexpr unsigned SENT = 0xFFFFFFFFu;
constexpr int SC1 = 16;
constexpr size_t O_WT = 0;
constexpr size_t O_XT = O_WT + 14336ull * 448 * 2;
constexpr size_t O_KT = O_XT + 64ull * 28672;
constexpr size_t O_VT = O_KT + 64ull * 16 * 28672;
constexpr size_t O_RT = O_VT + 64ull * 16 * 28672;
constexpr size_t O_UT = O_RT + 64ull * 114688;
constexpr size_t O_HT = O_UT + 64ull * 114688;
constexpr size_t O_GT = O_HT + 64ull * 114688;
constexpr size_t O_HO = O_GT + 64ull * 28672;
constexpr unsigned HO_STEP = 40960, HO_MEM = 0, HO_SP = 8192, HO_EX = 24576;
constexpr unsigned HO_BYTES = 65 * HO_STEP;
constexpr unsigned SPIN_LIMIT = 400000;

DEV bf16x8 asb(u32x4 v) { return __builtin_bit_cast(bf16x8, v); }
DEV f32x16 mfma32(u32x4 a, u32x4 b, f32x16 c) { return __builtin_amdgcn_mfma_f32_32x32x16_bf16(asb(a), asb(b), c, 0, 0, 0); }
DEV f32x4 mfma16(u32x4 a, u32x4 b, f32x4 c) { return __builtin_amdgcn_mfma_f32_16x16x32_bf16(asb(a), asb(b), c, 0, 0, 0); }
DEV f32x4 mfma16k16(u32x2 a, u32x2 b, f32x4 c) { return __builtin_amdgcn_mfma_f32_16x16x16bf16_1k(__builtin_bit_cast(s16x4, a), __builtin_bit_cast(s16x4, b), c, 0, 0, 0); }
DEV unsigned pk(float a, float b) { f32x2 t = {a, b}; return __builtin_bit_cast(unsigned, __builtin_convertvector(t, bf16x2)); }
DEV float blo(unsigned v) { return __uint_as_float(v << 16); }
DEV float bhi(unsigned v) { return __uint_as_float(v & 0xFFFF0000u); }
DEV float rbf(float a) { return blo(pk(a, a)); }
DEV int perm32(int r) { return (r & 19) | ((r & 4) << 1) | ((r & 8) >> 1); }
DEV unsigned ror8(unsigned v) { return __builtin_amdgcn_mov_dpp(v, 0x128, 0xf, 0xf, true); }
DEV float ror8f(float v) { return __uint_as_float(ror8(__float_as_uint(v))); }
DEV u32x4 ror8v(u32x4 v) { u32x4 r; r.x = ror8(v.x); r.y = ror8(v.y); r.z = ror8(v.z); r.w = ror8(v.w); return r; }
DEV float max16(float v) { auto r = __builtin_amdgcn_permlane16_swap(__float_as_uint(v), __float_as_uint(v), false, false); return fmaxf(__uint_as_float(r[0]), __uint_as_float(r[1])); }
DEV float sum16(float v) { auto r = __builtin_amdgcn_permlane16_swap(__float_as_uint(v), __float_as_uint(v), false, false); return __uint_as_float(r[0]) + __uint_as_float(r[1]); }
DEV float max32(float v) { auto r = __builtin_amdgcn_permlane32_swap(__float_as_uint(v), __float_as_uint(v), false, false); return fmaxf(__uint_as_float(r[0]), __uint_as_float(r[1])); }
DEV float sum32(float v) { auto r = __builtin_amdgcn_permlane32_swap(__float_as_uint(v), __float_as_uint(v), false, false); return __uint_as_float(r[0]) + __uint_as_float(r[1]); }
DEV u32x4 pk8(const f32x16& a, int o) { u32x4 r; r.x = pk(a[o], a[o + 1]); r.y = pk(a[o + 2], a[o + 3]); r.z = pk(a[o + 4], a[o + 5]); r.w = pk(a[o + 6], a[o + 7]); return r; }
DEV u32x4 ldg(const void* p) { return *(const GAS u32x4*)p; }
DEV void stg(void* p, u32x4 v) { *(GAS u32x4*)p = v; }
DEV u32x4 lds128(unsigned a) { return *(LAS u32x4*)(uintptr_t)a; }
DEV f32x4 lds128f(unsigned a) { return *(LAS f32x4*)(uintptr_t)a; }
DEV void sts128(unsigned a, u32x4 v) { *(LAS u32x4*)(uintptr_t)a = v; }
DEV void sts128f(unsigned a, f32x4 v) { *(LAS f32x4*)(uintptr_t)a = v; }
DEV u32x2 ldstr(unsigned a) { return __builtin_bit_cast(u32x2, __builtin_amdgcn_ds_read_tr16_b64_v4i16((LAS s16x4*)(uintptr_t)a)); }
DEV void glds16(const void* g, unsigned l) { __builtin_amdgcn_global_load_lds((const GAS void*)g, (LAS void*)(uintptr_t)l, 16, 0, 0); }
DEV void bar() { __builtin_amdgcn_s_barrier(); }
DEV void fence_c() { asm volatile("" ::: "memory"); }
#define WAIT_LGKM0() asm volatile("s_waitcnt lgkmcnt(0)" ::: "memory")
#define WAIT_VM(n) asm volatile("s_waitcnt vmcnt(" #n ")" ::: "memory")
DEV unsigned umax4(u32x4 v) { return max(max(v.x, v.y), max(v.z, v.w)); }
DEV unsigned nosent(unsigned v) { return min(v, 0xFFFFFFFEu); }

DEV void stage_kn(const u16* w, int ld, int nI, unsigned l, int lane) {
    for (int i = 0; i < nI; ++i) glds16(w + (size_t)(16 * i + (lane >> 2)) * ld + 8 * (lane & 3), l + i * 1024);
}
DEV u32x4 frag_tr(unsigned l, int ks, int lane, bool swz) {
    const int g = lane >> 4, q = (lane >> 2) & 3, p = lane & 3;
    const int pp = swz ? (((p & 1) << 1) | (p >> 1)) : p;
    const unsigned a = l + (16 * ks + 8 * (g >> 1) + q) * 64 + (g & 1) * 32 + pp * 8;
    u32x2 x = ldstr(a), y = ldstr(a + 256);
    u32x4 r; r.x = x.x; r.y = x.y; r.z = y.x; r.w = y.y; return r;
}

__global__ void __launch_bounds__(256, 1) k_prep(const u16* __restrict__ x, const u16* __restrict__ mem0, const u16* __restrict__ Wq, const u16* __restrict__ Wk,
                                                 const u16* __restrict__ Wv, const u16* __restrict__ Wo, unsigned char* __restrict__ ws) {
    const int b = blockIdx.x, tid = threadIdx.x, lane = tid & 63, w = __builtin_amdgcn_readfirstlane(tid >> 6);
    const int idx = b >> 3, mat = (idx >> 3) * 8 + (b & 7), sub = idx & 7, eh = sub & 1, ng = sub >> 1;
    const int h = mat & 15;
    const bool isC = mat >= 16;
    const int nt0 = ng < 2 ? ng * 4 : 8 + (ng - 2) * 3, ntn = ng < 2 ? 4 : 3;
    const bool act = w < ntn;
    const int nt = act ? nt0 + w : nt0;
    const u16* We = isC ? Wv : Wk;
    for (int i = 0; i < 28; ++i) {
        const int id = w * 28 + i, rb = id >> 2, kq = id & 3;
        const int row = rb * 8 + (lane >> 3), m = row & 31;
        const int e = eh * 224 + (row & ~31) + perm32(m);
        const int ch = (lane & 7) ^ ((m >> 1) & 7);
        glds16(We + (size_t)e * 4096 + h * 256 + kq * 64 + ch * 8, id * 1024);
    }
    u32x4 nf[16];
    const unsigned so = 114688 + w * 8192;
    if (!isC) {
#pragma unroll
        for (int ks = 0; ks < 16; ++ks) nf[ks] = ldg(Wq + (size_t)(nt * 32 + (lane & 31)) * 4096 + h * 256 + 16 * ks + 8 * (lane >> 5));
    } else {
        const u16* src = Wo + (size_t)h * 256 * 448 + nt * 32;
        stage_kn(src, 448, 8, so, lane);
#pragma unroll
        for (int i = 0; i < 8; ++i) nf[8 + i] = ldg(src + (size_t)(128 + 16 * i + (lane >> 2)) * 448 + 8 * (lane & 3));
    }
    for (int rr = 0; rr < 2; ++rr) {
        const int row = b * 8 + w * 2 + rr;
        const bool on = lane < 56;
        u32x4 v = {0, 0, 0, 0};
        if (on) v = ldg(x + (size_t)row * 448 + lane * 8);
        float f[8] = {blo(v.x), bhi(v.x), blo(v.y), bhi(v.y), blo(v.z), bhi(v.z), blo(v.w), bhi(v.w)};
        float s = 0;
#pragma unroll
        for (int i = 0; i < 8; ++i) s += f[i];
#pragma unroll
        for (int o = 32; o; o >>= 1) s += __shfl_xor(s, o);
        const float mean = s * (1.0f / 448.0f);
        float q = 0;
#pragma unroll
        for (int i = 0; i < 8; ++i) { const float d = f[i] - mean; q += on ? d * d : 0.0f; }
#pragma unroll
        for (int o = 32; o; o >>= 1) q += __shfl_xor(q, o);
        const float mb = rbf(mean), vb = rbf(q * (1.0f / 448.0f));
        const float sd = rbf(sqrtf(rbf(vb + 1e-5f)));
        u32x4 o4;
#pragma unroll
        for (int i = 0; i < 8; ++i) f[i] = rbf(f[i] - mb) / sd;
        o4.x = pk(f[0], f[1]); o4.y = pk(f[2], f[3]); o4.z = pk(f[4], f[5]); o4.w = pk(f[6], f[7]);
        if (on) stg(ws + O_XT + (size_t)(row >> 5) * 28672 + (lane >> 1) * 1024 + ((lane & 1) * 32 + (row & 31)) * 16, o4);
    }
    {
        const u32x4 sv = {SENT, SENT, SENT, SENT};
        for (unsigned o = (b * 256 + tid) * 16; o < HO_BYTES; o += 65536 * 16) {
            u32x4 v = sv;
            if (o < 7168) {
                const int c = o >> 8, i = (o >> 5) & 7, hh = (o >> 4) & 1;
                v = ldg(mem0 + i * 448 + c * 16 + hh * 8);
                v.x = nosent(v.x); v.y = nosent(v.y); v.z = nosent(v.z); v.w = nosent(v.w);
            }
            stg(ws + O_HO + o, v);
        }
    }
    WAIT_VM(0);
    if (isC) {
#pragma unroll
        for (int ks = 0; ks < 8; ++ks) nf[ks] = frag_tr(so, ks, lane, false);
        WAIT_LGKM0();
#pragma unroll
        for (int i = 0; i < 8; ++i) sts128(so + i * 1024 + lane * 16, nf[8 + i]);
        WAIT_LGKM0();
#pragma unroll
        for (int ks = 0; ks < 8; ++ks) nf[8 + ks] = frag_tr(so, ks, lane, false);
    }
    bar();
    if (!act) return;
    const float sc = isC ? 1.0f : 1.4426950408889634f / 16.0f;
    const int m = lane & 31, hf = lane >> 5;
    u16* dst = (u16*)(ws + O_WT) + (size_t)((isC ? 7168 : 0) + h * 448 + nt * 32 + m) * 448 + eh * 224 + 8 * hf;
    for (int j = 0; j < 7; ++j) {
        f32x16 acc = {};
        const unsigned base = (j * 4 + (m >> 3)) * 4096 + (m & 7) * 128;
#pragma unroll
        for (int ks = 0; ks < 16; ++ks) {
            const int ch = ((2 * ks + hf) & 7) ^ ((m >> 1) & 7);
            acc = mfma32(lds128(base + (ks >> 2) * 1024 + ch * 16), nf[ks], acc);
        }
#pragma unroll
        for (int i = 0; i < 16; ++i) acc[i] *= sc;
        stg(dst + 32 * j, pk8(acc, 0));
        stg(dst + 32 * j + 16, pk8(acc, 8));
    }
}

DEV void ring_issue(const unsigned char* ws, int seg, int slot, int w, int lane) {
    const unsigned char* src = ws + O_XT + (size_t)seg * 28672 + w * 7168 + lane * 16;
#pragma unroll
    for (int i = 0; i < 7; ++i) glds16(src + i * 1024, slot * 28672 + w * 7168 + i * 1024);
}

template <bool VPART>
DEV void gemm_body(unsigned char* ws, int T, int w, int lane) {
    const int h = T / 14, j = T % 14, m = lane & 31, hf = lane >> 5;
    for (int s = 0; s < 3; ++s) ring_issue(ws, s, s, w, lane);
    u32x4 wf[28];
    const u16* wsrc = (const u16*)(ws + O_WT) + (size_t)((VPART ? 7168 : 0) + h * 448 + 32 * j + (VPART ? m : perm32(m))) * 448 + 8 * hf;
#pragma unroll
    for (int ks = 0; ks < 28; ++ks) wf[ks] = ldg(wsrc + 16 * ks);
#pragma unroll
    for (int ks = 0; ks < 28; ++ks) asm volatile("" : "+v"(wf[ks]));
    unsigned char* dst = VPART ? ws + O_VT + ((size_t)((2 * j + (m >> 4)) * 16 + h) * 4 + hf) * 256 + (m & 15) * 16
                               : ws + O_KT + (size_t)h * 28672 + j * 2048 + lane * 16;
    for (int s = 0; s < NSEG; ++s) {
        WAIT_VM(14);
        bar();
        ring_issue(ws, min(s + 3, NSEG - 1), (s + 3) & 3, w, lane);
        const unsigned base = (s & 3) * 28672 + lane * 16;
        f32x16 acc = {};
#pragma unroll
        for (int ks = 0; ks < 28; ++ks) {
            const u32x4 xf = lds128(base + ks * 1024);
            acc = VPART ? mfma32(xf, wf[ks], acc) : mfma32(wf[ks], xf, acc);
        }
        unsigned char* d = dst + (size_t)s * 458752;
        stg(d, pk8(acc, 0));
        stg(d + (VPART ? 512 : 1024), pk8(acc, 8));
    }
    WAIT_VM(0);
}

DEV void gluh_body(unsigned char* ws, const u16* Wg, const u16* Wu, int ft, int s0, int s1, int w, int lane) {
    u32x4 gf[28], uf[28];
    const unsigned so = w * 28672;
    stage_kn(Wg + ft * 32, FF, 28, so, lane);
    WAIT_VM(0);
#pragma unroll
    for (int ks = 0; ks < 28; ++ks) gf[ks] = frag_tr(so, ks, lane, true);
    WAIT_LGKM0();
#pragma unroll
    for (int ks = 0; ks < 28; ++ks) asm volatile("" : "+v"(gf[ks]));
    stage_kn(Wu + ft * 32, FF, 28, so, lane);
    WAIT_VM(0);
#pragma unroll
    for (int ks = 0; ks < 28; ++ks) uf[ks] = frag_tr(so, ks, lane, true);
    WAIT_LGKM0();
#pragma unroll
    for (int ks = 0; ks < 28; ++ks) asm volatile("" : "+v"(uf[ks]));
    bar();
    for (int s = 0; s < 3; ++s) ring_issue(ws, min(s0 + s, s1 - 1), s, w, lane);
    for (int s = s0; s < s1; ++s) {
        WAIT_VM(14);
        bar();
        ring_issue(ws, min(s + 3, s1 - 1), (s - s0 + 3) & 3, w, lane);
        const unsigned base = ((s - s0) & 3) * 28672 + lane * 16;
        f32x16 ag = {}, au = {};
#pragma unroll
        for (int ks = 0; ks < 28; ++ks) {
            const u32x4 xf = lds128(base + ks * 1024);
            ag = mfma32(gf[ks], xf, ag);
            au = mfma32(uf[ks], xf, au);
        }
#pragma unroll
        for (int i = 0; i < 16; ++i) ag[i] = rbf(ag[i]) * rbf(au[i]);
        unsigned char* d = ws + O_HT + (size_t)s * 114688 + ft * 2048 + lane * 16;
        stg(d, pk8(ag, 0));
        stg(d + 1024, pk8(ag, 8));
    }
    WAIT_VM(0);
}

__global__ void __launch_bounds__(256, 1) k_gemm(unsigned char* __restrict__ ws, const u16* __restrict__ Wg, const u16* __restrict__ Wu) {
    const int b = blockIdx.x, lane = threadIdx.x & 63, w = __builtin_amdgcn_readfirstlane(threadIdx.x >> 6);
    if (b < 56) gemm_body<false>(ws, b * 4 + w, w, lane);
    else if (b < 112) gemm_body<true>(ws, (b - 56) * 4 + w, w, lane);
    else { const int q = b - 112, fg = q % 14, sp = q / 14; gluh_body(ws, Wg, Wu, fg * 4 + w, sp * 16, sp * 16 + 16, w, lane); }
}

DEV void glu2_body(unsigned char* ws, const u16* Wd, int cp, int s0, int s1, int w, int lane) {
    u32x4 d0[28], d1[28];
    const unsigned so = w * 28672;
    stage_kn(Wd + (size_t)w * 448 * 448 + cp * 64, 448, 28, so, lane);
    WAIT_VM(0);
#pragma unroll
    for (int ks = 0; ks < 28; ++ks) d0[ks] = frag_tr(so, ks, lane, true);
    WAIT_LGKM0();
#pragma unroll
    for (int ks = 0; ks < 28; ++ks) asm volatile("" : "+v"(d0[ks]));
    stage_kn(Wd + (size_t)w * 448 * 448 + cp * 64 + 32, 448, 28, so, lane);
    WAIT_VM(0);
#pragma unroll
    for (int ks = 0; ks < 28; ++ks) d1[ks] = frag_tr(so, ks, lane, true);
    WAIT_LGKM0();
#pragma unroll
    for (int ks = 0; ks < 28; ++ks) asm volatile("" : "+v"(d1[ks]));
    bar();
    for (int s = s0; s < s1; ++s) {
        const unsigned char* src = ws + O_HT + (size_t)s * 114688 + w * 28672 + lane * 16;
        u32x4 hf[28];
#pragma unroll
        for (int ks = 0; ks < 28; ++ks) hf[ks] = ldg(src + ks * 1024);
        f32x16 a0 = {}, a1 = {};
#pragma unroll
        for (int ks = 0; ks < 28; ++ks) { a0 = mfma32(d0[ks], hf[ks], a0); a1 = mfma32(d1[ks], hf[ks], a1); }
        const unsigned bb = (s & 1) * 32768;
#pragma unroll
        for (int q = 0; q < 4; ++q) {
            sts128f(bb + w * 8192 + q * 1024 + lane * 16, f32x4{a0[4 * q], a0[4 * q + 1], a0[4 * q + 2], a0[4 * q + 3]});
            sts128f(bb + w * 8192 + (4 + q) * 1024 + lane * 16, f32x4{a1[4 * q], a1[4 * q + 1], a1[4 * q + 2], a1[4 * q + 3]});
        }
        WAIT_LGKM0();
        bar();
        f32x4 r0 = {}, r1 = {};
#pragma unroll
        for (int ww = 0; ww < 4; ++ww) { r0 += lds128f(bb + ww * 8192 + (2 * w) * 1024 + lane * 16); r1 += lds128f(bb + ww * 8192 + (2 * w + 1) * 1024 + lane * 16); }
        u32x4 o; o.x = pk(r0[0], r0[1]); o.y = pk(r0[2], r0[3]); o.z = pk(r1[0], r1[1]); o.w = pk(r1[2], r1[3]);
        stg(ws + O_GT + (size_t)s * 28672 + cp * 4096 + w * 1024 + lane * 16, o);
    }
}

__global__ void __launch_bounds__(256, 1) k_glu2(unsigned char* __restrict__ ws, const u16* __restrict__ Wd) {
    const int b = blockIdx.x, lane = threadIdx.x & 63, w = __builtin_amdgcn_readfirstlane(threadIdx.x >> 6);
    glu2_body(ws, Wd, b % 7, (b / 7) * 4, (b / 7) * 4 + 4, w, lane);
}

typedef __amdgpu_buffer_rsrc_t rsrc_t;
DEV u32x4 bld4(rsrc_t r, unsigned o, unsigned so) { return __builtin_amdgcn_raw_buffer_load_b128(r, o, so, SC1); }
DEV u32x2 bld2(rsrc_t r, unsigned o, unsigned so) { return __builtin_amdgcn_raw_buffer_load_b64(r, o, so, SC1); }
DEV void bst1(rsrc_t r, unsigned o, unsigned so, unsigned v) { __builtin_amdgcn_raw_buffer_store_b32(v, r, o, so, SC1); }
DEV void bst2(rsrc_t r, unsigned o, unsigned so, u32x2 v) { __builtin_amdgcn_raw_buffer_store_b64(v, r, o, so, SC1); }
DEV unsigned memoff(int i, int ch) { return (ch >> 1) * 256 + i * 32 + (ch & 1) * 16; }

template <int NK>
DEV void a_worker(unsigned char* ws, rsrc_t ho, int h, int j, int w, int lane) {
    const int n = lane & 15, kg = lane >> 4, i8 = n & 7, up = n >> 3;
    u32x4 wf[7][NK];
#pragma unroll
    for (int t = 0; t < 7; ++t) {
        const int d = t < 6 ? 32 * (t >> 1) + 8 * (n >> 2) + 4 * (t & 1) + (n & 3) : 96 + n;
        const u16* src = (const u16*)(ws + O_WT) + (size_t)(h * 448 + 112 * j + d) * 448 + 128 * w + 8 * kg;
#pragma unroll
        for (int q = 0; q < NK; ++q) wf[t][q] = ldg(src + 32 * q);
    }
    const unsigned ob0 = memoff(i8, 4 * (4 * w + up) + kg), ob1 = memoff(i8, 4 * (4 * w + 2 + up) + kg);
    const unsigned os0 = memoff(i8, 14 * j + 4 * up + kg), os1 = memoff(i8, up ? 14 * j + 12 + (kg >> 1) : 14 * j + 8 + kg);
    bool dead = false;
    for (int s = 0; s < NSEG; ++s) {
        const unsigned sb = s * HO_STEP;
        u32x4 b0, b1, s0, s1;
        for (unsigned spin = 0;; ++spin) {
            fence_c();
            b0 = bld4(ho, ob0, sb);
            if (NK > 2) b1 = bld4(ho, ob1, sb);
            s0 = bld4(ho, os0, sb);
            s1 = bld4(ho, os1, sb);
            unsigned mx = max(umax4(b0), max(umax4(s0), umax4(s1)));
            if (NK > 2) mx = max(mx, umax4(b1));
            if (__all(mx != SENT) || dead) break;
            if (spin > SPIN_LIMIT) { dead = true; break; }
        }
        u32x4 bf[4];
        bf[0] = b0; bf[1] = ror8v(b0);
        if (NK > 2) { bf[2] = b1; bf[3] = ror8v(b1); }
        f32x4 acc[7];
#pragma unroll
        for (int t = 0; t < 7; ++t) {
            acc[t] = f32x4{0, 0, 0, 0};
#pragma unroll
            for (int q = 0; q < NK; ++q) acc[t] = mfma16(wf[t][q], bf[q], acc[t]);
        }
        const u32x4 s0r = ror8v(s0), s1r = ror8v(s1);
        u32x2 s3; s3.x = (kg & 1) ? s1r.z : s1r.x; s3.y = (kg & 1) ? s1r.w : s1r.y;
        f32x4 sa = {0, 0, 0, 0};
#pragma unroll
        for (int p = 0; p < 3; ++p) {
            u32x4 ra; ra.x = pk(acc[2 * p][0], acc[2 * p][1]); ra.y = pk(acc[2 * p][2], acc[2 * p][3]);
            ra.z = pk(acc[2 * p + 1][0], acc[2 * p + 1][1]); ra.w = pk(acc[2 * p + 1][2], acc[2 * p + 1][3]);
            sa = mfma16(ra, p == 0 ? s0 : p == 1 ? s0r : s1, sa);
        }
        { u32x2 ra; ra.x = pk(acc[6][0], acc[6][1]); ra.y = pk(acc[6][2], acc[6][3]); sa = mfma16k16(ra, s3, sa); }
        if (n < 8 && kg < 2) sts128f(w * 256 + n * 32 + kg * 16, sa);
        WAIT_LGKM0();
        bar();
        if (w == 3) {
            const LAS float* p = (const LAS float*)(uintptr_t)(lane * 4);
            const float v = (p[0] + p[64]) + (p[128] + p[192]);
            bst1(ho, HO_SP + (h * 4 + j) * 256 + lane * 4, sb, nosent(__float_as_uint(v)));
        }
        if (n < 8) {
#pragma unroll
            for (int t = 0; t < 7; ++t) sts128f(1024 + w * 3712 + n * 464 + (t < 6 ? 32 * (t >> 1) + 8 * kg + 4 * (t & 1) : 96 + 4 * kg) * 4, acc[t]);
        }
        WAIT_LGKM0();
        bar();
        const int tid = w * 64 + lane;
        if (tid < 112) {
            const int key = tid & 7, kc = tid >> 3;
            f32x4 r0 = {}, r1 = {};
#pragma unroll
            for (int ww = 0; ww < 4; ++ww) { r0 += lds128f(1024 + ww * 3712 + key * 464 + kc * 32); r1 += lds128f(1024 + ww * 3712 + key * 464 + kc * 32 + 16); }
            u32x4 o; o.x = pk(r0[0], r0[1]); o.y = pk(r0[2], r0[3]); o.z = pk(r1[0], r1[1]); o.w = pk(r1[2], r1[3]);
            stg(ws + O_RT + (size_t)s * 114688 + (h >> 2) * 28672 + (14 * j + kc) * 512 + ((h & 3) * 8 + key) * 16, o);
        }
    }
}

template <int NK>
DEV void x_worker(unsigned char* ws, rsrc_t ho, int h, int w, int lane) {
    const int n = lane & 15, kg = lane >> 4, i8 = n & 7, up = n >> 3;
    const unsigned ob0 = memoff(i8, 4 * (4 * w + up) + kg), ob1 = memoff(i8, 4 * (4 * w + 2 + up) + kg);
    const int key0 = 16 * (n >> 3) + 4 * ((n >> 2) & 1) + (n & 3);
    const unsigned char* ksrc = ws + O_KT + (size_t)h * 28672 + (16 * w + kg) * 512 + key0 * 16;
    u32x4 kf[2][NK];
#pragma unroll
    for (int q = 0; q < NK; ++q) { kf[0][q] = ldg(ksrc + q * 2048); kf[1][q] = ldg(ksrc + q * 2048 + 128); }
    bool dead = false;
    for (int s = 0; s < NSEG - 1; ++s) {
        const unsigned sb = s * HO_STEP;
        u32x4 b0, b1;
        for (unsigned spin = 0;; ++spin) {
            fence_c();
            b0 = bld4(ho, ob0, sb);
            if (NK > 2) b1 = bld4(ho, ob1, sb);
            unsigned mx = umax4(b0);
            if (NK > 2) mx = max(mx, umax4(b1));
            if (__all(mx != SENT) || dead) break;
            if (spin > SPIN_LIMIT) { dead = true; break; }
        }
        u32x4 bf[4];
        bf[0] = b0; bf[1] = ror8v(b0);
        if (NK > 2) { bf[2] = b1; bf[3] = ror8v(b1); }
        f32x4 a0 = {0, 0, 0, 0}, a1 = {0, 0, 0, 0};
#pragma unroll
        for (int q = 0; q < NK; ++q) { a0 = mfma16(kf[0][q], bf[q], a0); a1 = mfma16(kf[1][q], bf[q], a1); }
        const unsigned lb = (s & 1) * 4096;
        if (n < 8) { sts128f(lb + w * 1024 + kg * 256 + n * 16, a0); sts128f(lb + w * 1024 + kg * 256 + 128 + n * 16, a1); }
        WAIT_LGKM0();
        bar();
        {
            const unsigned char* kn = ksrc + (size_t)(s + 1) * 458752;
#pragma unroll
            for (int q = 0; q < NK; ++q) { kf[0][q] = ldg(kn + q * 2048); kf[1][q] = ldg(kn + q * 2048 + 128); }
        }
        if (w == 3) {
            const f32x4 v = (lds128f(lb + lane * 16) + lds128f(lb + 1024 + lane * 16)) + (lds128f(lb + 2048 + lane * 16) + lds128f(lb + 3072 + lane * 16));
            float m = fmaxf(fmaxf(v[0], v[1]), fmaxf(v[2], v[3]));
            m = fmaxf(m, ror8f(m)); m = max16(m); m = max32(m);
            u32x2 e; e.x = pk(__builtin_amdgcn_exp2f(v[0] - m), __builtin_amdgcn_exp2f(v[1] - m)); e.y = pk(__builtin_amdgcn_exp2f(v[2] - m), __builtin_amdgcn_exp2f(v[3] - m));
            e.x = nosent(e.x); e.y = nosent(e.y);
            bst2(ho, HO_EX + h * 640 + i8 * 64 + kg * 16 + up * 8, sb, e);
            float l = (blo(e.x) + bhi(e.x)) + (blo(e.y) + bhi(e.y));
            l += ror8f(l); l = sum16(l); l = sum32(l);
            if (lane < 8) { u32x2 st; st.x = nosent(__float_as_uint(m)); st.y = nosent(__float_as_uint(l)); bst2(ho, HO_EX + h * 640 + 512 + lane * 8, sb, st); }
        }
    }
}

DEV void c_worker(unsigned char* ws, rsrc_t ho, int c, int w, int lane) {
    const int n = lane & 15, kg = lane >> 4, i8 = n & 7, up = n >> 3;
    u32x4 wf[4][14];
#pragma unroll
    for (int g = 0; g < 4; ++g) {
        const u16* src = (const u16*)(ws + O_WT) + (size_t)(7168 + (4 * w + g) * 448 + 16 * c + n) * 448 + 8 * kg;
#pragma unroll
        for (int ks = 0; ks < 14; ++ks) wf[g][ks] = ldg(src + 32 * ks);
    }
    const unsigned char* vsrc = ws + O_VT + (size_t)(c * 16 + 4 * w) * 1024 + lane * 16;
    u32x4 vf[4];
#pragma unroll
    for (int g = 0; g < 4; ++g) vf[g] = ldg(vsrc + g * 1024);
    const unsigned om0 = (w * 112 + lane) * 16, om1 = (w * 112 + (lane < 48 ? 64 + lane : lane)) * 16;
    const int hd = 4 * w + 2 * up + (kg >> 1);
    const unsigned oe0 = HO_EX + (4 * w + up) * 640 + i8 * 64 + kg * 16, oe1 = oe0 + 1280, ost = HO_EX + hd * 640 + 512 + i8 * 8;
    const unsigned osp = HO_SP + hd * 1024 + i8 * 32 + (kg & 1) * 16;
    const unsigned lrd = 4096 + (kg >> 1) * 256 + i8 * 32 + (kg & 1) * 16;
    const int dsel = i8 - 4 * (kg & 1);
    const bool lo = kg < 2;
    bool dead = false;
    for (int s = 0; s < NSEG; ++s) {
        const unsigned sb = s * HO_STEP;
        u32x4 m0, m1;
        for (unsigned spin = 0;; ++spin) {
            fence_c();
            m0 = bld4(ho, om0, sb);
            m1 = bld4(ho, om1, sb);
            if (__all(max(umax4(m0), umax4(m1)) != SENT) || dead) break;
            if (spin > SPIN_LIMIT) { dead = true; break; }
        }
        sts128(4096 + om0, m0);
        sts128(4096 + om1, m1);
        WAIT_LGKM0();
        bar();
        f32x4 u[4] = {};
#pragma unroll
        for (int ks = 0; ks < 14; ++ks) {
            const u32x4 mf = lds128(lrd + ks * 512);
#pragma unroll
            for (int g = 0; g < 4; ++g) u[g] = mfma16(mf, wf[g][ks], u[g]);
        }
        u32x4 A01, A23;
        {
            f32x4 p, q;
#pragma unroll
            for (int r = 0; r < 4; ++r) { p[r] = lo ? u[0][r] : u[1][r]; q[r] = lo ? u[2][r] : u[3][r]; }
            A01.x = pk(p[0], p[1]); A01.y = pk(p[2], p[3]); A23.x = pk(q[0], q[1]); A23.y = pk(q[2], q[3]);
            stg(ws + O_UT + (size_t)s * 114688 + c * 4096 + w * 1024 + lane * 16, u32x4{A01.x, A01.y, A23.x, A23.y});
        }
        if (s == NSEG - 1) break;
        u32x4 e0, e1; u32x2 st;
        for (unsigned spin = 0;; ++spin) {
            fence_c();
            e0 = bld4(ho, oe0, sb);
            e1 = bld4(ho, oe1, sb);
            st = bld2(ho, ost, sb);
            if (__all(max(max(umax4(e0), umax4(e1)), max(st.x, st.y)) != SENT) || dead) break;
            if (spin > SPIN_LIMIT) { dead = true; break; }
        }
        {
            const f32x4 z = {0, 0, 0, 0};
            const f32x4 o0 = mfma16(e0, vf[0], z), o1 = mfma16(e0, vf[1], z), o2 = mfma16(e1, vf[2], z), o3 = mfma16(e1, vf[3], z);
            f32x4 p, q;
#pragma unroll
            for (int r = 0; r < 4; ++r) { p[r] = lo ? o0[r] : o1[r]; q[r] = lo ? o2[r] : o3[r]; }
            A01.z = pk(p[0], p[1]); A01.w = pk(p[2], p[3]); A23.z = pk(q[0], q[1]); A23.w = pk(q[2], q[3]);
        }
        u32x4 p0, p1, p2, p3;
        for (unsigned spin = 0;; ++spin) {
            fence_c();
            p0 = bld4(ho, osp, sb); p1 = bld4(ho, osp + 256, sb); p2 = bld4(ho, osp + 512, sb); p3 = bld4(ho, osp + 768, sb);
            if (__all(max(max(umax4(p0), umax4(p1)), max(umax4(p2), umax4(p3))) != SENT) || dead) break;
            if (spin > SPIN_LIMIT) { dead = true; break; }
        }
        const f32x4 sv = (__builtin_bit_cast(f32x4, p0) + __builtin_bit_cast(f32x4, p1)) + (__builtin_bit_cast(f32x4, p2) + __builtin_bit_cast(f32x4, p3));
        const float mx = __uint_as_float(st.x), lx = __uint_as_float(st.y);
        const float m = max16(fmaxf(fmaxf(fmaxf(sv[0], sv[1]), fmaxf(sv[2], sv[3])), mx));
        const float q0 = __builtin_amdgcn_exp2f(sv[0] - m), q1 = __builtin_amdgcn_exp2f(sv[1] - m), q2 = __builtin_amdgcn_exp2f(sv[2] - m), q3 = __builtin_amdgcn_exp2f(sv[3] - m);
        const float fx = __builtin_amdgcn_exp2f(mx - m);
        const float inv = __builtin_amdgcn_rcpf(sum16((q0 + q1) + (q2 + q3)) + lx * fx);
        const float f = fx * inv;
        u32x4 B;
        B.x = pk(q0 * inv, q1 * inv); B.y = pk(q2 * inv, q3 * inv);
        B.z = pk(dsel == 0 ? f : 0.0f, dsel == 1 ? f : 0.0f); B.w = pk(dsel == 2 ? f : 0.0f, dsel == 3 ? f : 0.0f);
        const f32x4 z = {0, 0, 0, 0};
        f32x4 d1 = mfma16(A01, B, z);
        const f32x4 d2 = mfma16(A23, B, z);
#pragma unroll
        for (int r = 0; r < 4; ++r) d1[r] += ror8f(d2[r]);
        if (n < 8) sts128f(w * 512 + n * 64 + kg * 16, d1);
        WAIT_LGKM0();
        bar();
        if (w == 0) {
            const LAS f32x2* p = (const LAS f32x2*)(uintptr_t)(lane * 8);
            const f32x2 v = (p[0] + p[64]) + (p[128] + p[192]);
            bst1(ho, HO_MEM + c * 256 + lane * 4, sb + HO_STEP, nosent(pk(v[0], v[1])));
        }
        {
            const unsigned char* vn = vsrc + (size_t)(s + 1) * 458752;
#pragma unroll
            for (int g = 0; g < 4; ++g) vf[g] = ldg(vn + g * 1024);
        }
    }
}

__global__ void __launch_bounds__(256, 1) k_chain(unsigned char* __restrict__ ws) {
    const int b = blockIdx.x, lane = threadIdx.x & 63, w = __builtin_amdgcn_readfirstlane(threadIdx.x >> 6);
    if ((b & 7) >= 4) return;
    const int id = (b >> 3) * 4 + (b & 3);
    const rsrc_t ho = __builtin_amdgcn_make_buffer_rsrc(ws + O_HO, 0, HO_BYTES, 0x00020000);
    if (id < 28) c_worker(ws, ho, id, w, lane);
    else if (id < 92) { const int a = id - 28; if (w == 3) a_worker<2>(ws, ho, a >> 2, a & 3, w, lane); else a_worker<4>(ws, ho, a >> 2, a & 3, w, lane); }
    else if (id < 108) { if (w == 3) x_worker<2>(ws, ho, id - 92, w, lane); else x_worker<4>(ws, ho, id - 92, w, lane); }
}

__global__ void __launch_bounds__(256, 1) k_final(const unsigned char* __restrict__ ws, const u16* __restrict__ x, u16* __restrict__ out) {
    const int s = blockIdx.x, lane = threadIdx.x & 63, w = __builtin_amdgcn_readfirstlane(threadIdx.x >> 6);
    const int t = lane & 31, hf = lane >> 5;
    {
        u32x4 xf[28];
#pragma unroll
        for (int ks = 0; ks < 28; ++ks) xf[ks] = ldg(ws + O_XT + (size_t)s * 28672 + ks * 1024 + lane * 16);
        f32x16 sm = {};
        {
            const unsigned char* src = ws + O_RT + (size_t)s * 114688 + w * 28672 + lane * 16;
#pragma unroll
            for (int ks = 0; ks < 28; ++ks) sm = mfma32(ldg(src + ks * 1024), xf[ks], sm);
        }
        unsigned pm[4][2];
#pragma unroll
        for (int g = 0; g < 4; ++g) {
            const unsigned char* src = ws + O_KT + ((size_t)s * 16 + 4 * w + g) * 28672 + lane * 16;
            f32x16 sx = {};
#pragma unroll
            for (int ks = 0; ks < 28; ++ks) sx = mfma32(ldg(src + ks * 1024), xf[ks], sx);
            float m = fmaxf(fmaxf(sm[4 * g], sm[4 * g + 1]), fmaxf(sm[4 * g + 2], sm[4 * g + 3]));
#pragma unroll
            for (int r = 0; r < 16; ++r) { const int key = 8 * (r >> 2) + 4 * hf + (r & 3); if (key > t) sx[r] = -INFINITY; m = fmaxf(m, sx[r]); }
            m = max32(m);
            float l = 0, em[4];
#pragma unroll
            for (int r = 0; r < 16; ++r) { sx[r] = __builtin_amdgcn_exp2f(sx[r] - m); l += sx[r]; }
#pragma unroll
            for (int r = 0; r < 4; ++r) { em[r] = __builtin_amdgcn_exp2f(sm[4 * g + r] - m); l += em[r]; }
            const float inv = 1.0f / sum32(l);
#pragma unroll
            for (int r = 0; r < 16; ++r) sx[r] *= inv;
            sts128(((4 * w + g) * 2) * 1024 + lane * 16, pk8(sx, 0));
            sts128(((4 * w + g) * 2 + 1) * 1024 + lane * 16, pk8(sx, 8));
            pm[g][0] = pk(em[0] * inv, em[1] * inv); pm[g][1] = pk(em[2] * inv, em[3] * inv);
        }
        sts128(32768 + (2 * w) * 1024 + lane * 16, u32x4{pm[0][0], pm[0][1], pm[2][0], pm[2][1]});
        sts128(32768 + (2 * w + 1) * 1024 + lane * 16, u32x4{pm[1][0], pm[1][1], pm[3][0], pm[3][1]});
    }
    WAIT_LGKM0();
    bar();
    const int pc = perm32(t);
    for (int cc = w; cc < 14; cc += 4) {
        const int cb = 2 * cc + (pc >> 4);
        const unsigned char* vs = ws + O_VT + (size_t)s * 458752 + (size_t)cb * 16384 + hf * 256 + (pc & 15) * 16;
        const unsigned char* us = ws + O_UT + (size_t)s * 114688 + cb * 4096 + hf * 256 + (pc & 15) * 16;
        f32x16 acc = {};
#pragma unroll 16
        for (int k = 0; k < 32; ++k) acc = mfma32(ldg(vs + k * 512), lds128(k * 1024 + lane * 16), acc);
#pragma unroll
        for (int k = 0; k < 8; ++k) acc = mfma32(ldg(us + k * 512), lds128(32768 + k * 1024 + lane * 16), acc);
#pragma unroll
        for (int rg = 0; rg < 2; ++rg) {
            const size_t o = (size_t)(s * 32 + t) * 448 + 32 * cc + 16 * rg + 8 * hf;
            const u32x4 xv = ldg(x + o), gv = ldg(ws + O_GT + (size_t)s * 28672 + (cc * 2 + rg) * 1024 + lane * 16);
            u32x4 r;
#pragma unroll
            for (int q = 0; q < 4; ++q) r[q] = pk(acc[8 * rg + 2 * q] + blo(xv[q]) + blo(gv[q]), acc[8 * rg + 2 * q + 1] + bhi(xv[q]) + bhi(gv[q]));
            stg(out + o, r);
        }
    }
}

extern "C" void kernel_launch(void* const* d_in, const int* in_sizes, int n_in, void* d_out, int out_size, void* d_ws, size_t ws_size, hipStream_t stream) {
    const u16* x = (const u16*)d_in[0];
    unsigned char* ws = (unsigned char*)d_ws;
    (void)hipFuncSetAttribute((const void*)k_prep, hipFuncAttributeMaxDynamicSharedMemorySize, 147456);
    (void)hipFuncSetAttribute((const void*)k_gemm, hipFuncAttributeMaxDynamicSharedMemorySize, 114688);
    (void)hipFuncSetAttribute((const void*)k_glu2, hipFuncAttributeMaxDynamicSharedMemorySize, 114688);
    (void)hipFuncSetAttribute((const void*)k_chain, hipFuncAttributeMaxDynamicSharedMemorySize, 98304);
    k_prep<<<256, 256, 147456, stream>>>(x, (const u16*)d_in[1], (const u16*)d_in[2], (const u16*)d_in[3], (const u16*)d_in[4], (const u16*)d_in[5], ws);
    k_gemm<<<168, 256, 114688, stream>>>(ws, (const u16*)d_in[8], (const u16*)d_in[9]);
    k_glu2<<<112, 256, 114688, stream>>>(ws, (const u16*)d_in[10]);
    k_chain<<<256, 256, 98304, stream>>>(ws);
    k_final<<<64, 256, 40960, stream>>>(ws, x, (u16*)d_out);
}
```

```cpp
#include <hip/hip_runtime.h>
#include <hip/hip_bf16.h>
#include <stdint.h>

typedef __bf16 bf16x8 __attribute__((ext_vector_type(8)));
typedef __bf16 bf16x2 __attribute__((ext_vector_type(2)));
typedef short s16x4 __attribute__((ext_vector_type(4)));
typedef float f32x2 __attribute__((ext_vector_type(2)));
typedef float f32x4 __attribute__((ext_vector_type(4)));
typedef float f32x16 __attribute__((ext_vector_type(16)));
typedef unsigned u32x2 __attribute__((ext_vector_type(2)));
typedef unsigned u32x4 __attribute__((ext_vector_type(4)));
typedef unsigned short u16;
#define LAS __attribute__((address_space(3)))
#define GAS __attribute__((address_space(1)))
#define DEV __device__ __forceinline__
typedef __amdgpu_buffer_rsrc_t rsrc_t;

constexpr int DM = 448, NH = 16, NSEG = 64, FF = 1792;
constexpr unsigned SENT = 0xFFFFFFFFu;
constexpr int SC1 = 16;
constexpr size_t O_WT = 0;
constexpr size_t O_XT = O_WT + 14336ull * 448 * 2;
constexpr size_t O_KT = O_XT + 64ull * 28672;
constexpr size_t O_VT = O_KT + 64ull * 16 * 28672;
constexpr size_t O_RT = O_VT + 64ull * 16 * 28672;
constexpr size_t O_UT = O_RT + 64ull * 114688;
constexpr size_t O_HT = O_UT + 64ull * 114688;
constexpr size_t O_GT = O_HT + 64ull * 114688;
constexpr size_t O_PAR = O_GT + 64ull * 28672;
constexpr size_t O_FLG = O_PAR + 4096;
constexpr unsigned F_K = 0, F_V = 256, F_H = 512, F_G = 768, F_P = 1024, F_D = 1280, F_CNT = 2048, F_BYTES = 4096;
constexpr size_t O_DBG = O_FLG + 4096;
constexpr unsigned WS_SPAN = 128u << 20;
constexpr size_t O_HO = O_DBG + (1u << 20);
constexpr unsigned HO_STEP = 131072, HO_MEM = 0, HO_SP = 16384, HO_EX = 49152, HO_ST = 65536, HO_CAL = 114688;
constexpr unsigned HO_BYTES = 64 * HO_STEP;
#define STAMP(i)
#define STAMPS
#define CNT(i)
#define DUMP(id, s)
#define PARFLIP 0u
constexpr unsigned SPIN_LIMIT = 400000;

DEV bf16x8 asb(u32x4 v) { return __builtin_bit_cast(bf16x8, v); }
DEV f32x16 mfma32(u32x4 a, u32x4 b, f32x16 c) { return __builtin_amdgcn_mfma_f32_32x32x16_bf16(asb(a), asb(b), c, 0, 0, 0); }
DEV f32x4 mfma16(u32x4 a, u32x4 b, f32x4 c) { return __builtin_amdgcn_mfma_f32_16x16x32_bf16(asb(a), asb(b), c, 0, 0, 0); }
DEV f32x4 mfma16k16(u32x2 a, u32x2 b, f32x4 c) { return __builtin_amdgcn_mfma_f32_16x16x16bf16_1k(__builtin_bit_cast(s16x4, a), __builtin_bit_cast(s16x4, b), c, 0, 0, 0); }
DEV unsigned pk(float a, float b) { f32x2 t = {a, b}; return __builtin_bit_cast(unsigned, __builtin_convertvector(t, bf16x2)); }
DEV float blo(unsigned v) { return __uint_as_float(v << 16); }
DEV float bhi(unsigned v) { return __uint_as_float(v & 0xFFFF0000u); }
DEV float rbf(float a) { return blo(pk(a, a)); }
DEV int perm32(int r) { return (r & 19) | ((r & 4) << 1) | ((r & 8) >> 1); }
DEV unsigned ror8(unsigned v) { return __builtin_amdgcn_mov_dpp(v, 0x128, 0xf, 0xf, true); }
DEV float ror8f(float v) { return __uint_as_float(ror8(__float_as_uint(v))); }
DEV u32x4 ror8v(u32x4 v) { u32x4 r; r.x = ror8(v.x); r.y = ror8(v.y); r.z = ror8(v.z); r.w = ror8(v.w); return r; }
DEV float max16(float v) { auto r = __builtin_amdgcn_permlane16_swap(__float_as_uint(v), __float_as_uint(v), false, false); return fmaxf(__uint_as_float(r[0]), __uint_as_float(r[1])); }
DEV float sum16(float v) { auto r = __builtin_amdgcn_permlane16_swap(__float_as_uint(v), __float_as_uint(v), false, false); return __uint_as_float(r[0]) + __uint_as_float(r[1]); }
DEV float max32(float v) { auto r = __builtin_amdgcn_permlane32_swap(__float_as_uint(v), __float_as_uint(v), false, false); return fmaxf(__uint_as_float(r[0]), __uint_as_float(r[1])); }
DEV float sum32(float v) { auto r = __builtin_amdgcn_permlane32_swap(__float_as_uint(v), __float_as_uint(v), false, false); return __uint_as_float(r[0]) + __uint_as_float(r[1]); }
DEV u32x4 pk8(const f32x16& a, int o) { u32x4 r; r.x = pk(a[o], a[o + 1]); r.y = pk(a[o + 2], a[o + 3]); r.z = pk(a[o + 4], a[o + 5]); r.w = pk(a[o + 6], a[o + 7]); return r; }
DEV u32x4 ldg(const void* p) { return *(const GAS u32x4*)p; }
DEV void stg(void* p, u32x4 v) { *(GAS u32x4*)p = v; }
DEV u32x4 lds128(unsigned a) { return *(LAS u32x4*)(uintptr_t)a; }
DEV f32x4 lds128f(unsigned a) { return *(LAS f32x4*)(uintptr_t)a; }
DEV void sts128(unsigned a, u32x4 v) { *(LAS u32x4*)(uintptr_t)a = v; }
DEV void sts128f(unsigned a, f32x4 v) { *(LAS f32x4*)(uintptr_t)a = v; }
DEV u32x2 ldstr(unsigned a) { return __builtin_bit_cast(u32x2, __builtin_amdgcn_ds_read_tr16_b64_v4i16((LAS s16x4*)(uintptr_t)a)); }
DEV void glds16(const void* g, unsigned l) { __builtin_amdgcn_global_load_lds((const GAS void*)g, (LAS void*)(uintptr_t)l, 16, 0, 0); }
DEV void bar() { __builtin_amdgcn_s_barrier(); }
DEV void fence_c() { asm volatile("" ::: "memory"); }
#define WAIT_LGKM0() asm volatile("s_waitcnt lgkmcnt(0)" ::: "memory")
#define WAIT_VM(n) asm volatile("s_waitcnt vmcnt(" #n ")" ::: "memory")
DEV unsigned umax4(u32x4 v) { return max(max(v.x, v.y), max(v.z, v.w)); }
DEV unsigned nosent(unsigned v) { return min(v, 0xFFFFFFFEu); }

DEV u32x4 bld4(rsrc_t r, unsigned o, unsigned so) { return __builtin_amdgcn_raw_buffer_load_b128(r, o, so, SC1); }
DEV u32x2 bld2(rsrc_t r, unsigned o, unsigned so) { return __builtin_amdgcn_raw_buffer_load_b64(r, o, so, SC1); }
DEV unsigned bld1(rsrc_t r, unsigned o, unsigned so) { return __builtin_amdgcn_raw_buffer_load_b32(r, o, so, SC1); }
DEV void bst1(rsrc_t r, unsigned o, unsigned so, unsigned v) { __builtin_amdgcn_raw_buffer_store_b32(v, r, o, so, SC1); }
DEV void bst2(rsrc_t r, unsigned o, unsigned so, u32x2 v) { __builtin_amdgcn_raw_buffer_store_b64(v, r, o, so, SC1); }
DEV void bst4(rsrc_t r, unsigned o, unsigned so, u32x4 v) { __builtin_amdgcn_raw_buffer_store_b128(v, r, o, so, SC1); }
DEV unsigned cnt_add(unsigned char* ws, unsigned o) { return __hip_atomic_fetch_add((GAS unsigned*)(ws + O_FLG + F_CNT + o), 1u, __ATOMIC_RELAXED, __HIP_MEMORY_SCOPE_AGENT); }
DEV void flag_set(rsrc_t wr, unsigned o) { bst1(wr, o, (unsigned)O_FLG, 1u); }
DEV void arrive(unsigned char* ws, rsrc_t wr, unsigned o, unsigned n) { if (cnt_add(ws, o) == n - 1) flag_set(wr, o); }
DEV void flag_wait(rsrc_t wr, unsigned fo, int s, unsigned long long& known, unsigned& budget, int lane, int slp) {
    while (!((known >> s) & 1ull)) {
        fence_c();
        known = __ballot(bld1(wr, lane * 4, (unsigned)O_FLG + fo) != 0u);
        fence_c();
        if ((known >> s) & 1ull) break;
        if (budget == 0) break;
        --budget;
        if (slp) __builtin_amdgcn_s_sleep(16); else __builtin_amdgcn_s_sleep(2);
    }
}

DEV void stage_kn(const u16* w, int ld, int nI, unsigned l, int lane) {
    for (int i = 0; i < nI; ++i) glds16(w + (size_t)(16 * i + (lane >> 2)) * ld + 8 * (lane & 3), l + i * 1024);
}
DEV u32x4 frag_tr(unsigned l, int ks, int lane, bool swz) {
    const int g = lane >> 4, q = (lane >> 2) & 3, p = lane & 3;
    const int pp = swz ? (((p & 1) << 1) | (p >> 1)) : p;
    const unsigned a = l + (16 * ks + 8 * (g >> 1) + q) * 64 + (g & 1) * 32 + pp * 8;
    u32x2 x = ldstr(a), y = ldstr(a + 256);
    u32x4 r; r.x = x.x; r.y = x.y; r.z = y.x; r.w = y.y; return r;
}

__global__ void __launch_bounds__(256, 1) k_prep(const u16* __restrict__ x, const u16* __restrict__ mem0, const u16* __restrict__ Wq, const u16* __restrict__ Wk,
                                                 const u16* __restrict__ Wv, const u16* __restrict__ Wo, unsigned char* ws, unsigned char* hob) {
    const int b = blockIdx.x, tid = threadIdx.x, lane = tid & 63, w = __builtin_amdgcn_readfirstlane(tid >> 6);
    const int idx = b >> 3, mat = (idx >> 3) * 8 + (b & 7), sub = idx & 7, eh = sub & 1, ng = sub >> 1;
    const int h = mat & 15;
    const bool isC = mat >= 16;
    const int nt0 = ng < 2 ? ng * 4 : 8 + (ng - 2) * 3, ntn = ng < 2 ? 4 : 3;
    const bool act = w < ntn;
    const int nt = act ? nt0 + w : nt0;
    const u16* We = isC ? Wv : Wk;
    for (int i = 0; i < 28; ++i) {
        const int id = w * 28 + i, rb = id >> 2, kq = id & 3;
        const int row = rb * 8 + (lane >> 3), m = row & 31;
        const int e = eh * 224 + (row & ~31) + perm32(m);
        const int ch = (lane & 7) ^ ((m >> 1) & 7);
        glds16(We + (size_t)e * 4096 + h * 256 + kq * 64 + ch * 8, id * 1024);
    }
    u32x4 nf[16];
    const unsigned so = 114688 + w * 8192;
    if (!isC) {
#pragma unroll
        for (int ks = 0; ks < 16; ++ks) nf[ks] = ldg(Wq + (size_t)(nt * 32 + (lane & 31)) * 4096 + h * 256 + 16 * ks + 8 * (lane >> 5));
    } else {
        const u16* src = Wo + (size_t)h * 256 * 448 + nt * 32;
        stage_kn(src, 448, 8, so, lane);
#pragma unroll
        for (int i = 0; i < 8; ++i) nf[8 + i] = ldg(src + (size_t)(128 + 16 * i + (lane >> 2)) * 448 + 8 * (lane & 3));
    }
    for (int rr = 0; rr < 2; ++rr) {
        const int row = b * 8 + w * 2 + rr;
        const bool on = lane < 56;
        u32x4 v = {0, 0, 0, 0};
        if (on) v = ldg(x + (size_t)row * 448 + lane * 8);
        float f[8] = {blo(v.x), bhi(v.x), blo(v.y), bhi(v.y), blo(v.z), bhi(v.z), blo(v.w), bhi(v.w)};
        float s = 0;
#pragma unroll
        for (int i = 0; i < 8; ++i) s += f[i];
#pragma unroll
        for (int o = 32; o; o >>= 1) s += __shfl_xor(s, o);
        const float mean = s * (1.0f / 448.0f);
        float q = 0;
#pragma unroll
        for (int i = 0; i < 8; ++i) { const float d = f[i] - mean; q += on ? d * d : 0.0f; }
#pragma unroll
        for (int o = 32; o; o >>= 1) q += __shfl_xor(q, o);
        const float mb = rbf(mean), vb = rbf(q * (1.0f / 448.0f));
        const float sd = rbf(sqrtf(rbf(vb + 1e-5f)));
        u32x4 o4;
#pragma unroll
        for (int i = 0; i < 8; ++i) f[i] = rbf(f[i] - mb) / sd;
        o4.x = pk(f[0], f[1]); o4.y = pk(f[2], f[3]); o4.z = pk(f[4], f[5]); o4.w = pk(f[6], f[7]);
        if (on) stg(ws + O_XT + (size_t)(row >> 5) * 28672 + (lane >> 1) * 1024 + ((lane & 1) * 32 + (row & 31)) * 16, o4);
    }
    {
        const u32x4 sv = {SENT, SENT, SENT, SENT};
        if (b == 255 && tid < F_BYTES / 16) stg(ws + O_FLG + tid * 16, u32x4{0, 0, 0, 0});
        for (int i = 0; i < 5; ++i) {
            const unsigned q = ((b & 3) * 5 + i) * 4096 + tid * 16, o = q & 8191;
            u32x4 v = sv;
            if (b < 4 && q < 16384 && o < 7168) {
                const int c = o >> 8, r = (o >> 5) & 7, hh = (o >> 4) & 1;
                v = ldg(mem0 + r * 448 + c * 16 + hh * 8);
                v.x = nosent(v.x); v.y = nosent(v.y); v.z = nosent(v.z); v.w = nosent(v.w);
            }
            stg(hob + (size_t)(b >> 2) * HO_STEP + q, v);
        }
    }
    WAIT_VM(0);
    if (isC) {
#pragma unroll
        for (int ks = 0; ks < 8; ++ks) nf[ks] = frag_tr(so, ks, lane, false);
        WAIT_LGKM0();
#pragma unroll
        for (int i = 0; i < 8; ++i) sts128(so + i * 1024 + lane * 16, nf[8 + i]);
        WAIT_LGKM0();
#pragma unroll
        for (int ks = 0; ks < 8; ++ks) nf[8 + ks] = frag_tr(so, ks, lane, false);
    }
    bar();
    if (!act) {
        if ((b & 7) == 0 && (idx & 7) == 4 && lane == 0) {
            const unsigned xcc = __builtin_amdgcn_s_getreg(20 | (3 << 11)) & 7u;
            const int pg = idx >> 3;
            const rsrc_t r = __builtin_amdgcn_make_buffer_rsrc(hob, 0, HO_BYTES, 0x00020000);
            unsigned best[2] = {~0u, ~0u};
            for (int i = 0; i < 14; ++i) {
                unsigned long long t0, t1;
                asm volatile("s_memtime %0\n\ts_waitcnt lgkmcnt(0)" : "=s"(t0) :: "memory");
                __builtin_amdgcn_raw_buffer_store_b32(i, r, (pg << 21) + HO_CAL + (i & 1) * 8192, 0, SC1);
                asm volatile("s_waitcnt vmcnt(0)\n\ts_memtime %0\n\ts_waitcnt lgkmcnt(0)" : "=s"(t1) :: "memory");
                const unsigned d = (unsigned)(t1 - t0);
                if (i & 1) best[1] = min(best[1], d); else best[0] = min(best[0], d);
            }
            ((unsigned*)(ws + O_PAR))[pg] = (best[1] < best[0]) != (xcc >= 4) ? 8192u : 0u;
            ((unsigned*)(ws + O_PAR))[4 + pg] = xcc;
            ((unsigned*)(ws + O_PAR))[8 + 2 * pg] = best[0]; ((unsigned*)(ws + O_PAR))[9 + 2 * pg] = best[1];
        }
        return;
    }
    const float sc = isC ? 1.0f : 1.4426950408889634f / 16.0f;
    const int m = lane & 31, hf = lane >> 5;
    u16* dst = (u16*)(ws + O_WT) + (size_t)((isC ? 7168 : 0) + h * 448 + nt * 32 + m) * 448 + eh * 224 + 8 * hf;
    for (int j = 0; j < 7; ++j) {
        f32x16 acc = {};
        const unsigned base = (j * 4 + (m >> 3)) * 4096 + (m & 7) * 128;
#pragma unroll
        for (int ks = 0; ks < 16; ++ks) {
            const int ch = ((2 * ks + hf) & 7) ^ ((m >> 1) & 7);
            acc = mfma32(lds128(base + (ks >> 2) * 1024 + ch * 16), nf[ks], acc);
        }
#pragma unroll
        for (int i = 0; i < 16; ++i) acc[i] *= sc;
        stg(dst + 32 * j, pk8(acc, 0));
        stg(dst + 32 * j + 16, pk8(acc, 8));
    }
}

DEV void ring_issue(const unsigned char* ws, int seg, int slot, int w, int lane) {
    const unsigned char* src = ws + O_XT + (size_t)seg * 28672 + w * 7168 + lane * 16;
#pragma unroll
    for (int i = 0; i < 7; ++i) glds16(src + i * 1024, slot * 28672 + w * 7168 + i * 1024);
}

template <bool VPART>
DEV void gemm_body(unsigned char* ws, rsrc_t wr, int T, int w, int lane) {
    const int h = T / 14, j = T % 14, m = lane & 31, hf = lane >> 5;
    for (int s = 0; s < 3; ++s) ring_issue(ws, s, s, w, lane);
    u32x4 wf[28];
    const u16* wsrc = (const u16*)(ws + O_WT) + (size_t)((VPART ? 7168 : 0) + h * 448 + 32 * j + (VPART ? m : perm32(m))) * 448 + 8 * hf;
#pragma unroll
    for (int ks = 0; ks < 28; ++ks) wf[ks] = ldg(wsrc + 16 * ks);
#pragma unroll
    for (int ks = 0; ks < 28; ++ks) asm volatile("" : "+a"(wf[ks]));
    const unsigned dst = VPART ? (unsigned)O_VT + (((2 * j + (m >> 4)) * 16 + h) * 4 + hf) * 256 + (m & 15) * 16
                               : (unsigned)O_KT + h * 28672 + j * 2048 + lane * 16;
    const unsigned fo = VPART ? F_V : F_K;
    for (int s = 0; s < NSEG; ++s) {
        WAIT_VM(9);
        bar();
        if (s >= 3 && w == 0 && lane == 0) cnt_add(ws, fo + (s - 2) * 4);
        const LAS u32x4* base = (const LAS u32x4*)(uintptr_t)((s & 3) * 28672 + lane * 16);
        u32x4 xf[28];
#pragma unroll
        for (int ks = 0; ks < 28; ++ks) xf[ks] = base[ks * 64];
        ring_issue(ws, min(s + 3, NSEG - 1), (s + 3) & 3, w, lane);
        __builtin_amdgcn_sched_barrier(0);
        f32x16 acc = {};
#pragma unroll
        for (int ks = 0; ks < 28; ++ks) acc = VPART ? mfma32(xf[ks], wf[ks], acc) : mfma32(wf[ks], xf[ks], acc);
        bst4(wr, dst, s * 458752, pk8(acc, 0));
        bst4(wr, dst + (VPART ? 512 : 1024), s * 458752, pk8(acc, 8));
        if (s == 0) { WAIT_VM(0); bar(); if (w == 0 && lane == 0) arrive(ws, wr, fo, 56); }
#pragma unroll
        for (int ks = 0; ks < 28; ++ks) asm volatile("" : "+a"(wf[ks]));
    }
    WAIT_VM(0);
    bar();
    if (w == 0 && lane < 2) cnt_add(ws, fo + (NSEG - 2 + lane) * 4);
}

DEV void load_wt(u32x4 (&f)[28], const u16* src, int ld, unsigned so, int lane) {
    stage_kn(src, ld, 28, so, lane);
    WAIT_VM(0);
#pragma unroll
    for (int ks = 0; ks < 28; ++ks) f[ks] = frag_tr(so, ks, lane, true);
    WAIT_LGKM0();
#pragma unroll
    for (int ks = 0; ks < 28; ++ks) asm volatile("" : "+a"(f[ks]));
}

DEV void gluh_body(unsigned char* ws, rsrc_t wr, const u16* Wg, const u16* Wu, int ft, int s0, int s1, int w, int lane) {
    u32x4 gf[28], uf[28];
    load_wt(gf, Wg + ft * 32, FF, w * 28672, lane);
    load_wt(uf, Wu + ft * 32, FF, w * 28672, lane);
    bar();
    for (int s = 0; s < 3; ++s) ring_issue(ws, min(s0 + s, s1 - 1), s, w, lane);
    for (int s = s0; s < s1; ++s) {
        WAIT_VM(14);
        bar();
        const LAS u32x4* base = (const LAS u32x4*)(uintptr_t)(((s - s0) & 3) * 28672 + lane * 16);
        u32x4 xf[28];
#pragma unroll
        for (int ks = 0; ks < 28; ++ks) xf[ks] = base[ks * 64];
        ring_issue(ws, min(s + 3, s1 - 1), (s - s0 + 3) & 3, w, lane);
        __builtin_amdgcn_sched_barrier(0);
        f32x16 ag = {}, au = {};
#pragma unroll
        for (int ks = 0; ks < 28; ++ks) { ag = mfma32(gf[ks], xf[ks], ag); au = mfma32(uf[ks], xf[ks], au); }
#pragma unroll
        for (int i = 0; i < 16; ++i) ag[i] = rbf(ag[i]) * rbf(au[i]);
        const unsigned d = (unsigned)O_HT + ft * 2048 + lane * 16;
        bst4(wr, d, s * 114688, pk8(ag, 0));
        bst4(wr, d + 1024, s * 114688, pk8(ag, 8));
#pragma unroll
        for (int ks = 0; ks < 28; ++ks) { asm volatile("" : "+a"(gf[ks])); asm volatile("" : "+a"(uf[ks])); }
    }
    WAIT_VM(0);
    bar();
    if (w == 0 && lane < s1 - s0) arrive(ws, wr, F_H + (s0 + lane) * 4, 14);
}

DEV void glu2_body(unsigned char* ws, rsrc_t wr, const u16* Wd, int cp, int s0, int s1, int w, int lane, unsigned& budget) {
    u32x4 d0[28], d1[28];
    load_wt(d0, Wd + (size_t)w * 448 * 448 + cp * 64, 448, w * 28672, lane);
    load_wt(d1, Wd + (size_t)w * 448 * 448 + cp * 64 + 32, 448, w * 28672, lane);
    bar();
    unsigned long long known = 0;
    for (int s = s0; s < s1; ++s) {
        flag_wait(wr, F_H, s, known, budget, lane, 1);
        const unsigned src = (unsigned)O_HT + w * 28672 + lane * 16;
        u32x4 hf[28];
#pragma unroll
        for (int ks = 0; ks < 28; ++ks) hf[ks] = bld4(wr, src + ks * 1024, s * 114688);
        __builtin_amdgcn_sched_barrier(0);
        f32x16 a0 = {}, a1 = {};
#pragma unroll
        for (int ks = 0; ks < 28; ++ks) { a0 = mfma32(d0[ks], hf[ks], a0); a1 = mfma32(d1[ks], hf[ks], a1); }
        const unsigned bb = (s & 1) * 32768;
#pragma unroll
        for (int q = 0; q < 4; ++q) {
            sts128f(bb + w * 8192 + q * 1024 + lane * 16, f32x4{a0[4 * q], a0[4 * q + 1], a0[4 * q + 2], a0[4 * q + 3]});
            sts128f(bb + w * 8192 + (4 + q) * 1024 + lane * 16, f32x4{a1[4 * q], a1[4 * q + 1], a1[4 * q + 2], a1[4 * q + 3]});
        }
        WAIT_LGKM0();
        bar();
        f32x4 r0 = {}, r1 = {};
#pragma unroll
        for (int ww = 0; ww < 4; ++ww) { r0 += lds128f(bb + ww * 8192 + (2 * w) * 1024 + lane * 16); r1 += lds128f(bb + ww * 8192 + (2 * w + 1) * 1024 + lane * 16); }
        u32x4 o; o.x = pk(r0[0], r0[1]); o.y = pk(r0[2], r0[3]); o.z = pk(r1[0], r1[1]); o.w = pk(r1[2], r1[3]);
        bst4(wr, (unsigned)O_GT + cp * 4096 + w * 1024 + lane * 16, s * 28672, o);
#pragma unroll
        for (int ks = 0; ks < 28; ++ks) { asm volatile("" : "+a"(d0[ks])); asm volatile("" : "+a"(d1[ks])); }
    }
    WAIT_VM(0);
    bar();
    if (w == 0 && lane < s1 - s0) arrive(ws, wr, F_G + (s0 + lane) * 4, 7);
}

#define LD4(d, o) "buffer_load_dwordx4 " d ", " o ", %[rs], %[sb] offen sc1\n\t"
DEV void ld_2(u32x4 rs, unsigned sb, unsigned o0, unsigned o1, u32x4& a, u32x4& b) {
    asm volatile(LD4("%0", "%2") LD4("%1", "%3") "s_waitcnt vmcnt(0)" : "=&v"(a), "=&v"(b) : "v"(o0), "v"(o1), [rs] "s"(rs), [sb] "s"(sb) : "memory");
}
DEV void ld_1(u32x4 rs, unsigned sb, unsigned o0, u32x4& a) {
    asm volatile(LD4("%0", "%1") "s_waitcnt vmcnt(0)" : "=&v"(a) : "v"(o0), [rs] "s"(rs), [sb] "s"(sb) : "memory");
}
DEV void ld_3(u32x4 rs, unsigned sb, unsigned o0, unsigned o1, unsigned o2, u32x4& a, u32x4& b, u32x4& c) {
    asm volatile(LD4("%0", "%3") LD4("%1", "%4") LD4("%2", "%5") "s_waitcnt vmcnt(0)" : "=&v"(a), "=&v"(b), "=&v"(c) : "v"(o0), "v"(o1), "v"(o2), [rs] "s"(rs), [sb] "s"(sb) : "memory");
}
DEV void ld_4(u32x4 rs, unsigned sb, unsigned o0, unsigned o1, unsigned o2, unsigned o3, u32x4& a, u32x4& b, u32x4& c, u32x4& d) {
    asm volatile(LD4("%0", "%4") LD4("%1", "%5") LD4("%2", "%6") LD4("%3", "%7") "s_waitcnt vmcnt(0)" : "=&v"(a), "=&v"(b), "=&v"(c), "=&v"(d) : "v"(o0), "v"(o1), "v"(o2), "v"(o3), [rs] "s"(rs), [sb] "s"(sb) : "memory");
}
DEV void ld_e(u32x4 rs, unsigned sb, unsigned o0, unsigned o2, u32x4& a, u32x4& b, u32x2& c) {
    asm volatile(LD4("%0", "%3") "buffer_load_dwordx4 %1, %3, %[rs], %[sb] offen offset:1024 sc1\n\tbuffer_load_dwordx2 %2, %4, %[rs], %[sb] offen sc1\n\ts_waitcnt vmcnt(0)"
                 : "=&v"(a), "=&v"(b), "=&v"(c) : "v"(o0), "v"(o2), [rs] "s"(rs), [sb] "s"(sb) : "memory");
}
DEV void ld_sp(u32x4 rs, unsigned sb, unsigned o, u32x4& a, u32x4& b, u32x4& c, u32x4& d) {
    asm volatile(LD4("%0", "%4") "buffer_load_dwordx4 %1, %4, %[rs], %[sb] offen offset:256 sc1\n\tbuffer_load_dwordx4 %2, %4, %[rs], %[sb] offen offset:512 sc1\n\t"
                 "buffer_load_dwordx4 %3, %4, %[rs], %[sb] offen offset:768 sc1\n\ts_waitcnt vmcnt(0)"
                 : "=&v"(a), "=&v"(b), "=&v"(c), "=&v"(d) : "v"(o), [rs] "s"(rs), [sb] "s"(sb) : "memory");
}
#define KS_E 13
#define KS_S 13
#define RESLEEP 0
#define DLY_A 0
#define DLY_X 0
#define DLY_C 0
#define DLY_S 4
#define POLL(LOADS, MX) for (;;) { LOADS; if (__builtin_expect(__all((MX) != SENT), 1)) break; if (budget == 0) break; --budget; if (RESLEEP) __builtin_amdgcn_s_sleep(RESLEEP); }
DEV unsigned stepbase(int s, unsigned par) { return s * HO_STEP + (((par >> (s >> 4)) & 1u) << 13); }
DEV unsigned memoff(int i, int ch) { return (ch >> 1) * 256 + i * 32 + (ch & 1) * 16; }

template <int NK>
DEV void a_worker(unsigned char* ws, rsrc_t wr, rsrc_t ho, u32x4 rs, unsigned par, int h, int j, int w, int lane) {
    const int n = lane & 15, kg = lane >> 4, i8 = n & 7, up = n >> 3;
    u32x4 wf[7][NK];
#pragma unroll
    for (int t = 0; t < 7; ++t) {
        const int d = t < 6 ? 32 * (t >> 1) + 8 * (n >> 2) + 4 * (t & 1) + (n & 3) : 96 + n;
        const u16* src = (const u16*)(ws + O_WT) + (size_t)(h * 448 + 112 * j + d) * 448 + 128 * w + 8 * kg;
#pragma unroll
        for (int q = 0; q < NK; ++q) wf[t][q] = ldg(src + 32 * q);
    }
    const unsigned ob0 = memoff(i8, 4 * (4 * w + up) + kg), ob1 = memoff(i8, 4 * (4 * w + 2 + up) + kg);
    const unsigned os0 = memoff(i8, 14 * j + 4 * up + kg), os1 = memoff(i8, up ? 14 * j + 12 + (kg >> 1) : 14 * j + 8 + kg);
    unsigned budget = SPIN_LIMIT;
    for (int s = 0; s < NSEG; ++s) {
        const unsigned sb = stepbase(s, par);
        u32x4 b0, b1, s0, s1;
        STAMPS; STAMP(0);
        if (DLY_A) __builtin_amdgcn_s_sleep(DLY_A);
        if (NK > 2) { POLL(ld_4(rs, sb, ob0, ob1, os0, os1, b0, b1, s0, s1), max(max(umax4(b0), umax4(b1)), max(umax4(s0), umax4(s1)))); }
        else { POLL(ld_3(rs, sb, ob0, os0, os1, b0, s0, s1), max(umax4(b0), max(umax4(s0), umax4(s1)))); }
        STAMP(1);
        u32x4 bf[4];
        bf[0] = b0; bf[1] = ror8v(b0);
        if (NK > 2) { bf[2] = b1; bf[3] = ror8v(b1); }
        f32x4 acc[7];
#pragma unroll
        for (int t = 0; t < 7; ++t) {
            acc[t] = f32x4{0, 0, 0, 0};
#pragma unroll
            for (int q = 0; q < NK; ++q) acc[t] = mfma16(wf[t][q], bf[q], acc[t]);
        }
        const u32x4 s0r = ror8v(s0), s1r = ror8v(s1);
        u32x2 s3; s3.x = (kg & 1) ? s1r.z : s1r.x; s3.y = (kg & 1) ? s1r.w : s1r.y;
        f32x4 sa = {0, 0, 0, 0};
#pragma unroll
        for (int p = 0; p < 3; ++p) {
            u32x4 ra; ra.x = pk(acc[2 * p][0], acc[2 * p][1]); ra.y = pk(acc[2 * p][2], acc[2 * p][3]);
            ra.z = pk(acc[2 * p + 1][0], acc[2 * p + 1][1]); ra.w = pk(acc[2 * p + 1][2], acc[2 * p + 1][3]);
            sa = mfma16(ra, p == 0 ? s0 : p == 1 ? s0r : s1, sa);
        }
        { u32x2 ra; ra.x = pk(acc[6][0], acc[6][1]); ra.y = pk(acc[6][2], acc[6][3]); sa = mfma16k16(ra, s3, sa); }
        if (n < 8 && kg < 2) sts128f(w * 256 + n * 32 + kg * 16, sa);
        WAIT_LGKM0();
        STAMP(2);
        bar();
        STAMP(3);
        if (w == 3) {
            const LAS float* p = (const LAS float*)(uintptr_t)(lane * 4);
            const float v = (p[0] + p[64]) + (p[128] + p[192]);
            bst1(ho, HO_SP + (h >> 3) * 16384 + ((h & 7) * 4 + j) * 256 + lane * 4, sb, nosent(__float_as_uint(v)));
        }
        if (w == 2 && h == 0 && j == 0 && s >= 2 && lane == 0) bst1(wr, (unsigned)O_FLG + F_P + (s - 2) * 4, 0, 1u);
        STAMP(4);
        if (n < 8) {
#pragma unroll
            for (int t = 0; t < 7; ++t) sts128f(1024 + w * 3712 + n * 464 + (t < 6 ? 32 * (t >> 1) + 8 * kg + 4 * (t & 1) : 96 + 4 * kg) * 4, acc[t]);
        }
        WAIT_LGKM0();
        bar();
        const int tid = w * 64 + lane;
        if (tid < 112) {
            const int key = tid & 7, kc = tid >> 3;
            f32x4 r0 = {}, r1 = {};
#pragma unroll
            for (int ww = 0; ww < 4; ++ww) { r0 += lds128f(1024 + ww * 3712 + key * 464 + kc * 32); r1 += lds128f(1024 + ww * 3712 + key * 464 + kc * 32 + 16); }
            u32x4 o; o.x = pk(r0[0], r0[1]); o.y = pk(r0[2], r0[3]); o.z = pk(r1[0], r1[1]); o.w = pk(r1[2], r1[3]);
            bst4(wr, (unsigned)O_RT + (h >> 2) * 28672 + (14 * j + kc) * 512 + ((h & 3) * 8 + key) * 16, s * 114688, o);
        }
        STAMP(5);
        if (w == 0 || w == 3) DUMP(56 + (h * 4 + j) * 2 + (w == 3), s);
    }
    WAIT_VM(0);
    bar();
    if (w == 0 && lane == 0) arrive(ws, wr, F_D + j * 128, 23);
}

template <int NK>
DEV void x_worker(unsigned char* ws, rsrc_t wr, rsrc_t ho, u32x4 rs, unsigned par, int h, int w, int lane) {
    const int n = lane & 15, kg = lane >> 4, i8 = n & 7, up = n >> 3;
    const unsigned ob0 = memoff(i8, 4 * (4 * w + up) + kg), ob1 = memoff(i8, 4 * (4 * w + 2 + up) + kg);
    const int key0 = 16 * (n >> 3) + 4 * ((n >> 2) & 1) + (n & 3);
    const unsigned ksrc = (unsigned)O_KT + h * 28672 + (16 * w + kg) * 512 + key0 * 16;
    u32x4 kf[2][NK];
    unsigned budget = SPIN_LIMIT;
    unsigned long long known = 0;
    flag_wait(wr, F_K, 0, known, budget, lane, 0);
#pragma unroll
    for (int q = 0; q < NK; ++q) { kf[0][q] = bld4(wr, ksrc + q * 2048, 0); kf[1][q] = bld4(wr, ksrc + q * 2048 + 128, 0); }
    for (int s = 0; s < NSEG - 1; ++s) {
        const unsigned sb = stepbase(s, par);
        u32x4 b0, b1;
        STAMPS; STAMP(0);
        if (DLY_X) __builtin_amdgcn_s_sleep(DLY_X);
        if (NK > 2) { POLL(ld_2(rs, sb, ob0, ob1, b0, b1), max(umax4(b0), umax4(b1))); }
        else { POLL(ld_1(rs, sb, ob0, b0), umax4(b0)); }
        STAMP(1);
        u32x4 bf[4];
        bf[0] = b0; bf[1] = ror8v(b0);
        if (NK > 2) { bf[2] = b1; bf[3] = ror8v(b1); }
        f32x4 a0 = {0, 0, 0, 0}, a1 = {0, 0, 0, 0};
#pragma unroll
        for (int q = 0; q < NK; ++q) { a0 = mfma16(kf[0][q], bf[q], a0); a1 = mfma16(kf[1][q], bf[q], a1); }
        const unsigned lb = (s & 1) * 4096;
        if (n < 8) { sts128f(lb + w * 1024 + kg * 256 + n * 16, a0); sts128f(lb + w * 1024 + kg * 256 + 128 + n * 16, a1); }
        WAIT_LGKM0();
        bar();
        if (w == 3) {
            const f32x4 v = (lds128f(lb + lane * 16) + lds128f(lb + 1024 + lane * 16)) + (lds128f(lb + 2048 + lane * 16) + lds128f(lb + 3072 + lane * 16));
            float m = fmaxf(fmaxf(v[0], v[1]), fmaxf(v[2], v[3]));
            m = fmaxf(m, ror8f(m)); m = max16(m); m = max32(m);
            u32x2 e; e.x = pk(__builtin_amdgcn_exp2f(v[0] - m), __builtin_amdgcn_exp2f(v[1] - m)); e.y = pk(__builtin_amdgcn_exp2f(v[2] - m), __builtin_amdgcn_exp2f(v[3] - m));
            e.x = nosent(e.x); e.y = nosent(e.y);
            bst2(ho, HO_EX + h * 512 + i8 * 64 + kg * 16 + up * 8, sb, e);
            float l = (blo(e.x) + bhi(e.x)) + (blo(e.y) + bhi(e.y));
            l += ror8f(l); l = sum16(l); l = sum32(l);
            if (lane < 8) { u32x2 st; st.x = nosent(__float_as_uint(m)); st.y = nosent(__float_as_uint(l)); bst2(ho, HO_ST + h * 128 + lane * 8, sb, st); }
            STAMP(2);
            DUMP(184 + h, s);
        }
        {
            flag_wait(wr, F_K, s + 1, known, budget, lane, 0);
#pragma unroll
            for (int q = 0; q < NK; ++q) { kf[0][q] = bld4(wr, ksrc + q * 2048, (s + 1) * 458752); kf[1][q] = bld4(wr, ksrc + q * 2048 + 128, (s + 1) * 458752); }
        }
    }
}

DEV void c_worker(unsigned char* ws, rsrc_t wr, rsrc_t ho, u32x4 rs, unsigned par, int c, int w, int lane) {
    const int n = lane & 15, kg = lane >> 4, i8 = n & 7, up = n >> 3;
    u32x4 wf[4][14];
#pragma unroll
    for (int g = 0; g < 4; ++g) {
        const u16* src = (const u16*)(ws + O_WT) + (size_t)(7168 + (4 * w + g) * 448 + 16 * c + n) * 448 + 8 * kg;
#pragma unroll
        for (int ks = 0; ks < 14; ++ks) wf[g][ks] = ldg(src + 32 * ks);
    }
    const unsigned vsrc = (unsigned)O_VT + (c * 16 + 4 * w) * 1024 + lane * 16, udst = (unsigned)O_UT + c * 4096 + w * 1024 + lane * 16;
    unsigned budget = SPIN_LIMIT;
    unsigned long long known = 0;
    flag_wait(wr, F_V, 0, known, budget, lane, 0);
    unsigned fails = 0;
    u32x4 vf[4], vnx[4], uprev = {0, 0, 0, 0};
#pragma unroll
    for (int g = 0; g < 4; ++g) vf[g] = bld4(wr, vsrc + g * 1024, 0);
    const unsigned om0 = (w * 112 + lane) * 16, om1 = (w * 112 + (lane < 48 ? 64 + lane : lane)) * 16;
    const int hd = 4 * w + 2 * up + (kg >> 1);
    const unsigned oe0 = HO_EX + (4 * w + up) * 512 + i8 * 64 + kg * 16, ost = HO_ST + hd * 128 + i8 * 8;
    const unsigned osp = HO_SP + (hd >> 3) * 16384 + (hd & 7) * 1024 + i8 * 32 + (kg & 1) * 16;
    const unsigned lrd = 4096 + (kg >> 1) * 256 + i8 * 32 + (kg & 1) * 16;
    const int dsel = i8 - 4 * (kg & 1);
    const bool lo = kg < 2;
    for (int s = 0; s < NSEG; ++s) {
        const unsigned sb = stepbase(s, par);
        u32x4 m0, m1;
        STAMPS; STAMP(0);
        if (DLY_C) __builtin_amdgcn_s_sleep(DLY_C);
        POLL(ld_2(rs, sb, om0, om1, m0, m1), max(umax4(m0), umax4(m1)));
        STAMP(1);
        sts128(4096 + om0, m0);
        sts128(4096 + om1, m1);
        {
            __builtin_amdgcn_s_waitcnt(0x0F70);
            const int sn = min(s + 1, NSEG - 1);
            flag_wait(wr, F_V, sn, known, budget, lane, 0);
#pragma unroll
            for (int g = 0; g < 4; ++g) vnx[g] = bld4(wr, vsrc + g * 1024, sn * 458752);
            if (s > 0) bst4(wr, udst, (s - 1) * 114688, uprev);
        }
#pragma unroll
        for (int g = 0; g < 4; ++g)
#pragma unroll
            for (int ks = 0; ks < 14; ++ks) asm volatile("" : "+a"(wf[g][ks]));
        WAIT_LGKM0();
        bar();
        f32x4 u[4] = {};
        u32x4 mf[14];
#pragma unroll
        for (int ks = 0; ks < 14; ++ks) mf[ks] = ((const LAS u32x4*)(uintptr_t)lrd)[ks * 32];
        __builtin_amdgcn_sched_barrier(0);
        u32x4 e0, e1, p0, p1, p2, p3; u32x2 st;
#pragma unroll
        for (int ks = 0; ks < 14; ++ks) {
#pragma unroll
            for (int g = 0; g < 4; ++g) u[g] = mfma16(mf[ks], wf[g][ks], u[g]);
            if (ks == KS_E) { __builtin_amdgcn_sched_barrier(0); e0 = bld4(ho, oe0, sb); e1 = bld4(ho, oe0 + 1024, sb); st = bld2(ho, ost, sb); __builtin_amdgcn_sched_barrier(0); }
            if (ks == KS_S) { __builtin_amdgcn_sched_barrier(0); if (DLY_S) __builtin_amdgcn_s_sleep(DLY_S); p0 = bld4(ho, osp, sb); p1 = bld4(ho, osp + 256, sb); p2 = bld4(ho, osp + 512, sb); p3 = bld4(ho, osp + 768, sb); __builtin_amdgcn_sched_barrier(0); }
        }
        u32x4 A01, A23;
        {
            f32x4 p, q;
#pragma unroll
            for (int r = 0; r < 4; ++r) { p[r] = lo ? u[0][r] : u[1][r]; q[r] = lo ? u[2][r] : u[3][r]; }
            A01.x = pk(p[0], p[1]); A01.y = pk(p[2], p[3]); A23.x = pk(q[0], q[1]); A23.y = pk(q[2], q[3]);
            uprev = u32x4{A01.x, A01.y, A23.x, A23.y};
        }
        STAMP(2);
        if (s == NSEG - 1) { bst4(wr, udst, s * 114688, uprev); break; }
        asm volatile("" : "+v"(A01), "+v"(A23));
        if (__builtin_expect(!__all(max(max(umax4(e0), umax4(e1)), max(st.x, st.y)) != SENT), 0)) { CNT(0); POLL(ld_e(rs, sb, oe0, ost, e0, e1, st), max(max(umax4(e0), umax4(e1)), max(st.x, st.y))); }
        STAMP(3);
        {
            const f32x4 z = {0, 0, 0, 0};
            const f32x4 o0 = mfma16(e0, vf[0], z), o1 = mfma16(e0, vf[1], z), o2 = mfma16(e1, vf[2], z), o3 = mfma16(e1, vf[3], z);
            f32x4 p, q;
#pragma unroll
            for (int r = 0; r < 4; ++r) { p[r] = lo ? o0[r] : o1[r]; q[r] = lo ? o2[r] : o3[r]; }
            A01.z = pk(p[0], p[1]); A01.w = pk(p[2], p[3]); A23.z = pk(q[0], q[1]); A23.w = pk(q[2], q[3]);
        }
#pragma unroll
        for (int g = 0; g < 4; ++g) { vf[g] = vnx[g]; asm volatile("" : "+v"(vf[g])); }
        asm volatile("" : "+v"(A01), "+v"(A23));
        if (__builtin_expect(!__all(max(max(umax4(p0), umax4(p1)), max(umax4(p2), umax4(p3))) != SENT), 0)) { CNT(1);
            POLL(ld_sp(rs, sb, osp, p0, p1, p2, p3), max(max(umax4(p0), umax4(p1)), max(umax4(p2), umax4(p3)))); }
        STAMP(4);
        const f32x4 sv = (__builtin_bit_cast(f32x4, p0) + __builtin_bit_cast(f32x4, p1)) + (__builtin_bit_cast(f32x4, p2) + __builtin_bit_cast(f32x4, p3));
        const float mx = __uint_as_float(st.x), lx = __uint_as_float(st.y);
        const float m = max16(fmaxf(fmaxf(fmaxf(sv[0], sv[1]), fmaxf(sv[2], sv[3])), mx));
        const float q0 = __builtin_amdgcn_exp2f(sv[0] - m), q1 = __builtin_amdgcn_exp2f(sv[1] - m), q2 = __builtin_amdgcn_exp2f(sv[2] - m), q3 = __builtin_amdgcn_exp2f(sv[3] - m);
        const float fx = __builtin_amdgcn_exp2f(mx - m);
        const float inv = __builtin_amdgcn_rcpf(sum16((q0 + q1) + (q2 + q3)) + lx * fx);
        const float f = fx * inv;
        u32x4 B;
        B.x = pk(q0 * inv, q1 * inv); B.y = pk(q2 * inv, q3 * inv);
        B.z = pk(dsel == 0 ? f : 0.0f, dsel == 1 ? f : 0.0f); B.w = pk(dsel == 2 ? f : 0.0f, dsel == 3 ? f : 0.0f);
        const f32x4 z = {0, 0, 0, 0};
        f32x4 d1 = mfma16(A01, B, z);
        const f32x4 d2 = mfma16(A23, B, z);
#pragma unroll
        for (int r = 0; r < 4; ++r) d1[r] += ror8f(d2[r]);
        if (n < 8) sts128f(w * 512 + n * 64 + kg * 16, d1);
        WAIT_LGKM0();
        STAMP(5);
        bar();
        STAMP(6);
        if (w == 0) {
            const LAS f32x2* p = (const LAS f32x2*)(uintptr_t)(lane * 8);
            const f32x2 v = (p[0] + p[64]) + (p[128] + p[192]);
            bst1(ho, HO_MEM + c * 256 + lane * 4, stepbase(s + 1, par), nosent(pk(v[0], v[1])));
        }
        STAMP(7);
        if (w < 2) DUMP(c * 2 + w, s);
    }
    WAIT_VM(0);
    bar();
    if (w == 0 && lane == 0) arrive(ws, wr, F_D + (c & 3) * 128, 23);
}

DEV void final_body(unsigned char* ws, rsrc_t wr, const u16* x, u16* out, int s, int g2, int w, int lane, unsigned& budget) {
    const int t = lane & 31, hf = lane >> 5;
    unsigned long long known = 0;
    {
        u32x4 xf[28];
#pragma unroll
        for (int ks = 0; ks < 28; ++ks) xf[ks] = ldg(ws + O_XT + (size_t)s * 28672 + ks * 1024 + lane * 16);
#pragma unroll
        for (int ks = 0; ks < 28; ++ks) asm volatile("" : "+a"(xf[ks]));
        flag_wait(wr, F_K, s, known, budget, lane, 1);
        f32x16 sx[4];
        {
            const unsigned src = (unsigned)O_KT + 4 * w * 28672 + lane * 16;
            u32x4 ka[28], kb[28];
#pragma unroll
            for (int ks = 0; ks < 28; ++ks) ka[ks] = bld4(wr, src + ks * 1024, s * 458752);
#pragma unroll
            for (int g = 0; g < 4; g += 2) {
#pragma unroll
                for (int ks = 0; ks < 28; ++ks) kb[ks] = bld4(wr, src + (g + 1) * 28672 + ks * 1024, s * 458752);
                __builtin_amdgcn_sched_barrier(0);
                sx[g] = f32x16{};
#pragma unroll
                for (int ks = 0; ks < 28; ++ks) sx[g] = mfma32(ka[ks], xf[ks], sx[g]);
                __builtin_amdgcn_sched_barrier(0);
                if (g == 0) {
#pragma unroll
                    for (int ks = 0; ks < 28; ++ks) ka[ks] = bld4(wr, src + 2 * 28672 + ks * 1024, s * 458752);
                }
                __builtin_amdgcn_sched_barrier(0);
                sx[g + 1] = f32x16{};
#pragma unroll
                for (int ks = 0; ks < 28; ++ks) sx[g + 1] = mfma32(kb[ks], xf[ks], sx[g + 1]);
                __builtin_amdgcn_sched_barrier(0);
            }
        }
        known = 0; flag_wait(wr, F_V, s, known, budget, lane, 1);
        known = 0; flag_wait(wr, F_G, s, known, budget, lane, 1);
        if (s < NSEG - 2) { known = 0; flag_wait(wr, F_P, s, known, budget, lane, 1); }
        else {
            for (;;) {
                fence_c();
                const unsigned v = bld1(wr, (lane & 3) * 128, (unsigned)O_FLG + F_D);
                fence_c();
                if (__all(v != 0u) || budget == 0) break;
                --budget;
                __builtin_amdgcn_s_sleep(2);
            }
        }
        f32x16 sm = {};
        {
            const unsigned src = (unsigned)O_RT + w * 28672 + lane * 16;
            u32x4 ra[28];
#pragma unroll
            for (int ks = 0; ks < 28; ++ks) ra[ks] = bld4(wr, src + ks * 1024, s * 114688);
            __builtin_amdgcn_sched_barrier(0);
#pragma unroll
            for (int ks = 0; ks < 28; ++ks) sm = mfma32(ra[ks], xf[ks], sm);
        }
        unsigned pm[4][2];
#pragma unroll
        for (int g = 0; g < 4; ++g) {
            float m = fmaxf(fmaxf(sm[4 * g], sm[4 * g + 1]), fmaxf(sm[4 * g + 2], sm[4 * g + 3]));
#pragma unroll
            for (int r = 0; r < 16; ++r) { const int key = 8 * (r >> 2) + 4 * hf + (r & 3); if (key > t) sx[g][r] = -INFINITY; m = fmaxf(m, sx[g][r]); }
            m = max32(m);
            float l = 0, em[4];
#pragma unroll
            for (int r = 0; r < 16; ++r) { sx[g][r] = __builtin_amdgcn_exp2f(sx[g][r] - m); l += sx[g][r]; }
#pragma unroll
            for (int r = 0; r < 4; ++r) { em[r] = __builtin_amdgcn_exp2f(sm[4 * g + r] - m); l += em[r]; }
            const float inv = 1.0f / sum32(l);
#pragma unroll
            for (int r = 0; r < 16; ++r) sx[g][r] *= inv;
            sts128(((4 * w + g) * 2) * 1024 + lane * 16, pk8(sx[g], 0));
            sts128(((4 * w + g) * 2 + 1) * 1024 + lane * 16, pk8(sx[g], 8));
            pm[g][0] = pk(em[0] * inv, em[1] * inv); pm[g][1] = pk(em[2] * inv, em[3] * inv);
        }
        sts128(32768 + (2 * w) * 1024 + lane * 16, u32x4{pm[0][0], pm[0][1], pm[2][0], pm[2][1]});
        sts128(32768 + (2 * w + 1) * 1024 + lane * 16, u32x4{pm[1][0], pm[1][1], pm[3][0], pm[3][1]});
    }
    const int pc = perm32(t);
    bool first = true;
    for (int cc = 7 * g2 + w; cc < 7 * g2 + 7; cc += 4) {
        const int cb = 2 * cc + (pc >> 4);
        const unsigned vs = (unsigned)O_VT + cb * 16384 + hf * 256 + (pc & 15) * 16;
        const unsigned us = (unsigned)O_UT + cb * 4096 + hf * 256 + (pc & 15) * 16;
        u32x4 va[40];
#pragma unroll
        for (int k = 0; k < 32; ++k) va[k] = bld4(wr, vs + k * 512, s * 458752);
#pragma unroll
        for (int k = 0; k < 8; ++k) va[32 + k] = bld4(wr, us + k * 512, s * 114688);
        u32x4 xv[2], gv[2];
#pragma unroll
        for (int rg = 0; rg < 2; ++rg) {
            xv[rg] = ldg(x + (size_t)(s * 32 + t) * 448 + 32 * cc + 16 * rg + 8 * hf);
            gv[rg] = bld4(wr, (unsigned)O_GT + (cc * 2 + rg) * 1024 + lane * 16, s * 28672);
        }
        if (first) { WAIT_LGKM0(); bar(); first = false; }
        __builtin_amdgcn_sched_barrier(0);
        f32x16 acc = {};
        const LAS u32x4* pb = (const LAS u32x4*)(uintptr_t)(lane * 16);
#pragma unroll
        for (int k0 = 0; k0 < 40; k0 += 8) {
            u32x4 pf[8];
#pragma unroll
            for (int k = 0; k < 8; ++k) pf[k] = pb[(k0 + k) * 64];
#pragma unroll
            for (int k = 0; k < 8; ++k) acc = mfma32(va[k0 + k], pf[k], acc);
        }
#pragma unroll
        for (int rg = 0; rg < 2; ++rg) {
            u32x4 r;
#pragma unroll
            for (int q = 0; q < 4; ++q) r[q] = pk(acc[8 * rg + 2 * q] + blo(xv[rg][q]) + blo(gv[rg][q]), acc[8 * rg + 2 * q + 1] + bhi(xv[rg][q]) + bhi(gv[rg][q]));
            stg(out + (size_t)(s * 32 + t) * 448 + 32 * cc + 16 * rg + 8 * hf, r);
        }
    }
}

__global__ void __launch_bounds__(256, 1) k_main(unsigned char* ws, unsigned char* hob, const u16* __restrict__ x, const u16* __restrict__ Wg, const u16* __restrict__ Wu,
                                                 const u16* __restrict__ Wd, u16* __restrict__ out) {
    const int b = blockIdx.x, lane = threadIdx.x & 63, w = __builtin_amdgcn_readfirstlane(threadIdx.x >> 6);
    const int xv = (b + (int)((const unsigned*)(ws + O_PAR))[4]) & 7;
    const int id = (b >> 3) * 4 + (xv & 3);
    const rsrc_t wr = __builtin_amdgcn_make_buffer_rsrc(ws, 0, WS_SPAN, 0x00020000);
    if (xv < 4) {
        const rsrc_t ho = __builtin_amdgcn_make_buffer_rsrc(hob, 0, HO_BYTES, 0x00020000);
        const u32x4 rs = {(unsigned)(uintptr_t)hob, (unsigned)((uintptr_t)hob >> 32), HO_BYTES, 0x00020000u};
        const unsigned par = (unsigned)__ballot(((const unsigned*)(ws + O_PAR))[lane & 3] != PARFLIP) & 15u;
        if (id < 28) c_worker(ws, wr, ho, rs, par, id, w, lane);
        else if (id < 92) { const int a = id - 28; if (w == 3) a_worker<2>(ws, wr, ho, rs, par, a >> 2, a & 3, w, lane); else a_worker<4>(ws, wr, ho, rs, par, a >> 2, a & 3, w, lane); }
        else if (id < 108) { if (w == 3) x_worker<2>(ws, wr, ho, rs, par, id - 92, w, lane); else x_worker<4>(ws, wr, ho, rs, par, id - 92, w, lane); }
        return;
    }
    unsigned budget = SPIN_LIMIT;
#define SQ(i)
    SQ(0);
    if (id < 112) {
        if (id < 56) gemm_body<false>(ws, wr, id * 4 + w, w, lane);
        else gemm_body<true>(ws, wr, (id - 56) * 4 + w, w, lane);
        SQ(1);
        gluh_body(ws, wr, Wg, Wu, (id % 14) * 4 + w, (id / 14) * 8, (id / 14) * 8 + 8, w, lane);
        SQ(2);
        glu2_body(ws, wr, Wd, id % 7, (id / 7) * 4, (id / 7) * 4 + 4, w, lane, budget);
        SQ(3);
    }
    if (id == 127 && w < 2) {
        const unsigned fo = w ? F_V : F_K;
        bool done = false;
        while (budget) {
            fence_c();
            const bool now = bld1(wr, lane * 4, (unsigned)O_FLG + F_CNT + fo) == 56u;
            if (now && !done) flag_set(wr, fo + lane * 4);
            done = now;
            if (__all(done)) break;
            --budget;
            __builtin_amdgcn_s_sleep(4);
        }
    }
    final_body(ws, wr, x, out, id >> 1, id & 1, w, lane, budget);
}

extern "C" void kernel_launch(void* const* d_in, const int* in_sizes, int n_in, void* d_out, int out_size, void* d_ws, size_t ws_size, hipStream_t stream) {
    const u16* x = (const u16*)d_in[0];
    unsigned char* ws = (unsigned char*)d_ws;
    unsigned char* hob = (unsigned char*)(((uintptr_t)ws + O_HO + 0x1FFFFF) & ~(uintptr_t)0x1FFFFF);
    (void)hipFuncSetAttribute((const void*)k_prep, hipFuncAttributeMaxDynamicSharedMemorySize, 147456);
    (void)hipFuncSetAttribute((const void*)k_main, hipFuncAttributeMaxDynamicSharedMemorySize, 114688);
    k_prep<<<256, 256, 147456, stream>>>(x, (const u16*)d_in[1], (const u16*)d_in[2], (const u16*)d_in[3], (const u16*)d_in[4], (const u16*)d_in[5], ws, hob);
    k_main<<<256, 256, 114688, stream>>>(ws, hob, x, (const u16*)d_in[8], (const u16*)d_in[9], (const u16*)d_in[10], (u16*)d_out);
}
```

```cpp
#include <hip/hip_runtime.h>
#include <hip/hip_bf16.h>
#include <stdint.h>

typedef __bf16 bf16x8 __attribute__((ext_vector_type(8)));
typedef __bf16 bf16x2 __attribute__((ext_vector_type(2)));
typedef short s16x4 __attribute__((ext_vector_type(4)));
typedef float f32x2 __attribute__((ext_vector_type(2)));
typedef float f32x4 __attribute__((ext_vector_type(4)));
typedef float f32x16 __attribute__((ext_vector_type(16)));
typedef unsigned u32x2 __attribute__((ext_vector_type(2)));
typedef unsigned u32x4 __attribute__((ext_vector_type(4)));
typedef unsigned short u16;
#define LAS __attribute__((address_space(3)))
#define GAS __attribute__((address_space(1)))
#define DEV __device__ __forceinline__
typedef __amdgpu_buffer_rsrc_t rsrc_t;

constexpr int DM = 448, NH = 16, NSEG = 64, FF = 1792;
constexpr unsigned SENT = 0xFFFFFFFFu;
constexpr int SC1 = 16;
constexpr size_t O_WT = 0;
constexpr size_t O_XT = O_WT + 14336ull * 448 * 2;
constexpr size_t O_KT = O_XT + 64ull * 28672;
constexpr size_t O_VT = O_KT + 64ull * 16 * 28672;
constexpr size_t O_RT = O_VT + 64ull * 16 * 28672;
constexpr size_t O_UT = O_RT + 64ull * 114688;
constexpr size_t O_HT = O_UT + 64ull * 114688;
constexpr size_t O_GT = O_HT + 64ull * 114688;
constexpr size_t O_PAR = O_GT + 64ull * 28672;
constexpr size_t O_FLG = O_PAR + 4096;
constexpr unsigned F_K = 0, F_V = 0, F_R = 256, F_S = 1792, F_X = 6144, F_Z = 7168, F_H = 512, F_G = 768, F_P = 1024, F_D = 1280, F_CNT = 2048, F_U = 4096;
constexpr size_t O_DBG = O_FLG + 8192;
constexpr unsigned WS_SPAN = 128u << 20;
constexpr size_t O_HO = O_DBG + (1u << 20);
constexpr unsigned HO_STEP = 262144, HO_GRP = 65536, HO_ST = 7168, HO_SP = 16384, HO_EX = 32768, HO_TS = 49152, HO_CAL = HO_TS + 4096;
constexpr unsigned HO_BYTES = 64 * HO_STEP;
#define DUMP(id)
#define VDBG
#define FQ(r, i)
constexpr unsigned SPIN_LIMIT = 400000;
#define PA 11
#define PB 3
#define PC 6
#define NCAL 8
#define PFK 0
#define MS 2
#define FB 1
#define UA 57
#define PFD 32

DEV unsigned wtoff(int row, int ch) { return (row >> 4) * 14336 + ch * 256 + (row & 15) * 16; }
DEV bf16x8 asb(u32x4 v) { return __builtin_bit_cast(bf16x8, v); }
DEV f32x16 mfma32(u32x4 a, u32x4 b, f32x16 c) { return __builtin_amdgcn_mfma_f32_32x32x16_bf16(asb(a), asb(b), c, 0, 0, 0); }
DEV f32x4 mfma16(u32x4 a, u32x4 b, f32x4 c) { return __builtin_amdgcn_mfma_f32_16x16x32_bf16(asb(a), asb(b), c, 0, 0, 0); }
DEV f32x4 mfma16k16(u32x2 a, u32x2 b, f32x4 c) { return __builtin_amdgcn_mfma_f32_16x16x16bf16_1k(__builtin_bit_cast(s16x4, a), __builtin_bit_cast(s16x4, b), c, 0, 0, 0); }
DEV unsigned pk(float a, float b) { f32x2 t = {a, b}; return __builtin_bit_cast(unsigned, __builtin_convertvector(t, bf16x2)); }
DEV float blo(unsigned v) { return __uint_as_float(v << 16); }
DEV float bhi(unsigned v) { return __uint_as_float(v & 0xFFFF0000u); }
DEV float rbf(float a) { return blo(pk(a, a)); }
DEV int perm32(int r) { return (r & 19) | ((r & 4) << 1) | ((r & 8) >> 1); }
DEV unsigned ror8(unsigned v) { return __builtin_amdgcn_mov_dpp(v, 0x128, 0xf, 0xf, true); }
DEV float ror8f(float v) { return __uint_as_float(ror8(__float_as_uint(v))); }
DEV u32x4 ror8v(u32x4 v) { u32x4 r; r.x = ror8(v.x); r.y = ror8(v.y); r.z = ror8(v.z); r.w = ror8(v.w); return r; }
DEV float max16(float v) { auto r = __builtin_amdgcn_permlane16_swap(__float_as_uint(v), __float_as_uint(v), false, false); return fmaxf(__uint_as_float(r[0]), __uint_as_float(r[1])); }
DEV float sum16(float v) { auto r = __builtin_amdgcn_permlane16_swap(__float_as_uint(v), __float_as_uint(v), false, false); return __uint_as_float(r[0]) + __uint_as_float(r[1]); }
DEV float max32(float v) { auto r = __builtin_amdgcn_permlane32_swap(__float_as_uint(v), __float_as_uint(v), false, false); return fmaxf(__uint_as_float(r[0]), __uint_as_float(r[1])); }
DEV float sum32(float v) { auto r = __builtin_amdgcn_permlane32_swap(__float_as_uint(v), __float_as_uint(v), false, false); return __uint_as_float(r[0]) + __uint_as_float(r[1]); }
template <int C> DEV float dppf(float v) { return __uint_as_float(__builtin_amdgcn_mov_dpp(__float_as_uint(v), C, 0xf, 0xf, true)); }
DEV float wsum(float v) { v += dppf<0x128>(v); v += dppf<0x124>(v); v += dppf<0x122>(v); v += dppf<0x121>(v); return sum32(sum16(v)); }
DEV u32x4 pk8(const f32x16& a, int o) { u32x4 r; r.x = pk(a[o], a[o + 1]); r.y = pk(a[o + 2], a[o + 3]); r.z = pk(a[o + 4], a[o + 5]); r.w = pk(a[o + 6], a[o + 7]); return r; }
DEV u32x4 ldg(const void* p) { return *(const GAS u32x4*)p; }
DEV void stg(void* p, u32x4 v) { *(GAS u32x4*)p = v; }
DEV u32x4 lds128(unsigned a) { return *(LAS u32x4*)(uintptr_t)a; }
DEV f32x4 lds128f(unsigned a) { return *(LAS f32x4*)(uintptr_t)a; }
DEV void sts128(unsigned a, u32x4 v) { *(LAS u32x4*)(uintptr_t)a = v; }
DEV void sts128f(unsigned a, f32x4 v) { *(LAS f32x4*)(uintptr_t)a = v; }
DEV u32x2 ldstr(unsigned a) { return __builtin_bit_cast(u32x2, __builtin_amdgcn_ds_read_tr16_b64_v4i16((LAS s16x4*)(uintptr_t)a)); }
DEV void glds16(const void* g, unsigned l) { __builtin_amdgcn_global_load_lds((const GAS void*)g, (LAS void*)(uintptr_t)l, 16, 0, 0); }
DEV void bar() { __builtin_amdgcn_s_barrier(); }
DEV void fence_c() { asm volatile("" ::: "memory"); }
#define WAIT_LGKM0() asm volatile("s_waitcnt lgkmcnt(0)" ::: "memory")
#define WAIT_VM(n) asm volatile("s_waitcnt vmcnt(" #n ")" ::: "memory")
DEV unsigned umax4(u32x4 v) { return max(max(v.x, v.y), max(v.z, v.w)); }
DEV unsigned nosent(unsigned v) { return min(v, 0xFFFFFFFEu); }

DEV u32x4 bld4(rsrc_t r, unsigned o, unsigned so) { return __builtin_amdgcn_raw_buffer_load_b128(r, o, so, SC1); }
DEV u32x2 bld2(rsrc_t r, unsigned o, unsigned so) { return __builtin_amdgcn_raw_buffer_load_b64(r, o, so, SC1); }
DEV unsigned bld1(rsrc_t r, unsigned o, unsigned so) { return __builtin_amdgcn_raw_buffer_load_b32(r, o, so, SC1); }
DEV void bst1(rsrc_t r, unsigned o, unsigned so, unsigned v) { __builtin_amdgcn_raw_buffer_store_b32(v, r, o, so, SC1); }
DEV void bst2(rsrc_t r, unsigned o, unsigned so, u32x2 v) { __builtin_amdgcn_raw_buffer_store_b64(v, r, o, so, SC1); }
DEV void bst4(rsrc_t r, unsigned o, unsigned so, u32x4 v) { __builtin_amdgcn_raw_buffer_store_b128(v, r, o, so, SC1); }
DEV unsigned cnt_add(unsigned char* ws, unsigned o) { return __hip_atomic_fetch_add((GAS unsigned*)(ws + O_FLG + F_CNT + o), 1u, __ATOMIC_RELAXED, __HIP_MEMORY_SCOPE_AGENT); }
DEV void flag_set(rsrc_t wr, unsigned o) { bst1(wr, o, (unsigned)O_FLG, 1u); }
DEV void arrive(unsigned char* ws, rsrc_t wr, unsigned o, unsigned n) { if (cnt_add(ws, o) == n - 1) flag_set(wr, o); }
DEV void flag_wait(rsrc_t wr, unsigned fo, int s, unsigned long long& known, unsigned& budget, int lane, int slp) {
    while (!((known >> s) & 1ull)) {
        fence_c();
        known = __ballot(bld1(wr, lane * 4, (unsigned)O_FLG + fo) != 0u);
        fence_c();
        if ((known >> s) & 1ull) break;
        if (budget == 0) break;
        --budget;
        if (slp) __builtin_amdgcn_s_sleep(16); else __builtin_amdgcn_s_sleep(2);
    }
}

DEV void all_wait(rsrc_t wr, unsigned o, unsigned& budget, int slp) {
    for (;;) {
        fence_c();
        const unsigned v = bld1(wr, o, (unsigned)O_FLG);
        fence_c();
        if (__all(v != 0u) || budget == 0) break;
        --budget;
        if (slp) __builtin_amdgcn_s_sleep(16); else __builtin_amdgcn_s_sleep(2);
    }
}

DEV void stage_kn(const u16* w, int ld, int nI, unsigned l, int lane) {
    for (int i = 0; i < nI; ++i) glds16(w + (size_t)(16 * i + (lane >> 2)) * ld + 8 * (lane & 3), l + i * 1024);
}
DEV u32x4 frag_tr(unsigned l, int ks, int lane, bool swz) {
    const int g = lane >> 4, q = (lane >> 2) & 3, p = lane & 3;
    const int pp = swz ? (((p & 1) << 1) | (p >> 1)) : p;
    const unsigned a = l + (16 * ks + 8 * (g >> 1) + q) * 64 + (g & 1) * 32 + pp * 8;
    u32x2 x = ldstr(a), y = ldstr(a + 256);
    u32x4 r; r.x = x.x; r.y = x.y; r.z = y.x; r.w = y.y; return r;
}

__global__ void __launch_bounds__(256, 1) k_prep(const u16* __restrict__ x, const u16* __restrict__ mem0, const u16* __restrict__ Wq, const u16* __restrict__ Wk,
                                                 const u16* __restrict__ Wv, const u16* __restrict__ Wo, unsigned char* ws, unsigned char* hob) {
    const int b = blockIdx.x, tid = threadIdx.x, lane = tid & 63, w = __builtin_amdgcn_readfirstlane(tid >> 6);
    const int idx = b >> 3, mat = (idx >> 3) * 8 + (b & 7), sub = idx & 7, eh = sub & 1, ng = sub >> 1;
    const int h = mat & 15;
    const bool isC = mat >= 16;
    const int nt0 = ng < 2 ? ng * 4 : 8 + (ng - 2) * 3, ntn = ng < 2 ? 4 : 3;
    const bool act = w < ntn;
    const int nt = act ? nt0 + w : nt0;
    const u16* We = isC ? Wv : Wk;
#define PQ(i)
    PQ(0);
    u32x4 xr[2], mr[2];
    asm volatile("global_load_dwordx4 %0, %4, off\n\tglobal_load_dwordx4 %1, %4, off offset:896\n\tglobal_load_dwordx4 %2, %5, off\n\tglobal_load_dwordx4 %3, %6, off"
                 : "=&v"(xr[0]), "=&v"(xr[1]), "=&v"(mr[0]), "=&v"(mr[1])
                 : "v"(x + (size_t)(b * 8 + w * 2) * 448 + min(lane, 55) * 8), "v"(mem0 + ((lane >> 1) & 7) * 448 + (w * 4 + (lane >> 4)) * 16 + (lane & 1) * 8),
                   "v"(mem0 + ((lane >> 1) & 7) * 448 + (min(w + 4, 6) * 4 + (lane >> 4)) * 16 + (lane & 1) * 8) : "memory");
    u32x4 nf[16];
    const unsigned so = 114688 + w * 8192;
    {
        const u16* src = isC ? Wo + (size_t)(h * 256 + (lane >> 2)) * 448 + nt * 32 + 8 * (lane & 3) : Wq + (size_t)(nt * 32 + (lane & 31)) * 4096 + h * 256 + 8 * (lane >> 5);
        const int step = isC ? 16 * 448 : 16;
#pragma unroll
        for (int ks = 0; ks < 16; ++ks) nf[ks] = ldg(src + ks * step);
    }
    for (int i = 0; i < 28; ++i) {
        const int id = w * 28 + i, rb = id >> 2, kq = id & 3;
        const int row = rb * 8 + (lane >> 3), m = row & 31;
        const int e = eh * 224 + (row & ~31) + perm32(m);
        const int ch = (lane & 7) ^ ((m >> 1) & 7);
        glds16(We + (size_t)e * 4096 + h * 256 + kq * 64 + ch * 8, id * 1024);
    }
    PQ(1);
    asm volatile("s_waitcnt vmcnt(44)" : "+v"(xr[0]), "+v"(xr[1]), "+v"(mr[0]), "+v"(mr[1]));
    {
        const rsrc_t r = __builtin_amdgcn_make_buffer_rsrc(hob, 0, HO_BYTES, 0x00020000);
        const unsigned f = b < 4 ? 0u : SENT;
        bst4(r, HO_TS + (w >> 1) * 8192 + (w & 1) * 1024 + lane * 16, b * HO_GRP, u32x4{f, f, f, f});
#pragma unroll
        for (int i = 0; i < 12; ++i) {
            const int k = i * 4 + w;
            u32x4 v = {SENT, SENT, SENT, SENT};
            if (i < 4 && b < 4 && (k & 7) < 7) {
                v = mr[i & 1];
                v.x = nosent(v.x); v.y = nosent(v.y); v.z = nosent(v.z); v.w = nosent(v.w);
            }
            bst4(r, k * 1024 + lane * 16, b * HO_GRP, v);
        }
        if (b == 0 && tid == 0) { ((unsigned*)(ws + O_PAR))[64] = __builtin_amdgcn_s_getreg(20 | (3 << 11)) & 7u; ((unsigned*)(ws + O_PAR))[65] = (unsigned)__builtin_amdgcn_s_memrealtime(); }
    }
#pragma unroll
    for (int rr = 0; rr < 2; ++rr) {
        const int row = b * 8 + w * 2 + rr;
        const bool on = lane < 56;
        const u32x4 v = on ? xr[rr] : u32x4{0, 0, 0, 0};
        float f[8] = {blo(v.x), bhi(v.x), blo(v.y), bhi(v.y), blo(v.z), bhi(v.z), blo(v.w), bhi(v.w)};
        float s = 0;
#pragma unroll
        for (int i = 0; i < 8; ++i) s += f[i];
        s = wsum(s);
        const float mean = s * (1.0f / 448.0f);
        float q = 0;
#pragma unroll
        for (int i = 0; i < 8; ++i) { const float d = f[i] - mean; q += on ? d * d : 0.0f; }
        q = wsum(q);
        const float mb = rbf(mean), vb = rbf(q * (1.0f / 448.0f));
        const float sd = rbf(sqrtf(rbf(vb + 1e-5f)));
        u32x4 o4;
#pragma unroll
        for (int i = 0; i < 8; ++i) f[i] = rbf(f[i] - mb) / sd;
        o4.x = pk(f[0], f[1]); o4.y = pk(f[2], f[3]); o4.z = pk(f[4], f[5]); o4.w = pk(f[6], f[7]);
        if (on) stg(ws + O_XT + (size_t)(row >> 5) * 28672 + (lane >> 1) * 1024 + ((lane & 1) * 32 + (row & 31)) * 16, o4);
    }
    if (b >= 254) stg(ws + O_FLG + (b - 254) * 4096 + tid * 16, u32x4{0, 0, 0, 0});
    PQ(2);
    WAIT_VM(0);
    PQ(3);
    if (isC) {
#pragma unroll
        for (int r = 0; r < 16; r += 8) {
#pragma unroll
            for (int i = 0; i < 8; ++i) sts128(so + i * 1024 + lane * 16, nf[r + i]);
            WAIT_LGKM0();
#pragma unroll
            for (int ks = 0; ks < 8; ++ks) nf[r + ks] = frag_tr(so, ks, lane, false);
            WAIT_LGKM0();
        }
    }
    bar();
    PQ(4);
    if (!act) return;
    const float sc = isC ? 1.0f : 1.4426950408889634f / 16.0f;
    const int m = lane & 31, hf = lane >> 5;
    const rsrc_t wr = __builtin_amdgcn_make_buffer_rsrc(ws, 0, WS_SPAN, 0x00020000);
    const unsigned dst = (unsigned)O_WT + wtoff((isC ? 7168 : 0) + h * 448 + nt * 32 + m, eh * 28 + hf);
    unsigned eo[4];
#pragma unroll
    for (int q = 0; q < 4; ++q) eo[q] = (m >> 3) * 4096 + (m & 7) * 128 + (((2 * q + hf) ^ (m >> 1)) & 7) * 16;
    u32x4 ef[16];
#pragma unroll
    for (int ks = 0; ks < 16; ++ks) ef[ks] = lds128(eo[ks & 3] + (ks >> 2) * 1024);
#pragma unroll
    for (int j = 0; j < 7; ++j) {
        f32x16 acc = {};
#pragma unroll
        for (int ks = 0; ks < 16; ++ks) {
            acc = mfma32(ef[ks], nf[ks], acc);
            if (j < 6) ef[ks] = lds128(eo[ks & 3] + (ks >> 2) * 1024 + (j + 1) * 16384);
            __builtin_amdgcn_sched_barrier(0);
        }
#pragma unroll
        for (int i = 0; i < 16; ++i) acc[i] *= sc;
        bst4(wr, dst, 1024 * j, pk8(acc, 0));
        bst4(wr, dst + 512, 1024 * j, pk8(acc, 8));
    }
    PQ(5);
    PQ(6);
}

DEV void ring_issue(const unsigned char* ws, int seg, int slot, int w, int lane) {
    const unsigned char* src = ws + O_XT + (size_t)seg * 28672 + w * 7168 + lane * 16;
#pragma unroll
    for (int i = 0; i < 7; ++i) glds16(src + i * 1024, slot * 28672 + w * 7168 + i * 1024);
}

template <bool VPART>
DEV void gemm_pair(unsigned char* ws, rsrc_t wr, const u32x4 (&wf)[28], unsigned dst, int s0, int who, int w, int lane) {
    u32x4 xf[28];
#pragma unroll
    for (int ks = 0; ks < 28; ++ks) xf[ks] = lds128((s0 & 3) * 28672 + lane * 16 + ks * 1024);
#pragma unroll
    for (int i = 0; i < 2; ++i) {
        f32x16 acc = {};
#pragma unroll
        for (int ks = 0; ks < 28; ++ks) {
            acc = VPART ? mfma32(xf[ks], wf[ks], acc) : mfma32(wf[ks], xf[ks], acc);
            if (i == 0) xf[ks] = lds128(((s0 & 3) + 1) * 28672 + lane * 16 + ks * 1024);
            __builtin_amdgcn_sched_barrier(0);
        }
        bst4(wr, dst, (s0 + i) * 458752, pk8(acc, 0));
        bst4(wr, dst + (VPART ? 512 : 1024), (s0 + i) * 458752, pk8(acc, 8));
    }
    WAIT_LGKM0();
    WAIT_VM(0);
    bar();
    if (w == 3 && lane == 0) bst1(wr, F_Z + who * 4, (unsigned)O_FLG, s0 + 2);
}

template <bool helper>
DEV void gemm_body(unsigned char* ws, rsrc_t wr, int T, int w, int lane) {
    const int h = T / 14, j = T % 14, m = lane & 31, hf = lane >> 5;
    u32x4 kf[28], vf[28];
    const unsigned char* ksrc = ws + O_WT + wtoff(h * 448 + 32 * j + perm32(m), hf);
    const unsigned char* vsrc = ws + O_WT + wtoff(7168 + h * 448 + 32 * j + m, hf);
    const unsigned dv = (unsigned)O_VT + (((2 * j + (m >> 4)) * 16 + h) * 4 + hf) * 256 + (m & 15) * 16, dk = (unsigned)O_KT + h * 28672 + j * 2048 + lane * 16;
    for (int s = 0; s < 2; ++s) ring_issue(ws, s, s, w, lane);
#pragma unroll
    for (int ks = 0; ks < 28; ++ks) kf[ks] = ldg((helper ? vsrc : ksrc) + 512 * ks);
#pragma unroll
    for (int ks = 0; ks < 28; ++ks) asm volatile("" : "+a"(kf[ks]));
    WAIT_VM(0);
    bar();
    FQ(144 + T / 4 + (helper ? 56 : 0), 0);
    for (int s = 0; s < 6; s += 2) {
        for (int i = 2; i < 4; ++i) ring_issue(ws, s + i, (s + i) & 3, w, lane);
        gemm_pair<helper>(ws, wr, kf, helper ? dv : dk, s, T / 4 + (helper ? 56 : 0), w, lane);
        if (!helper && s < 4) { FQ(144 + T / 4, 1 + s / 2); }
    }
    if (helper) return;
#pragma unroll
    for (int ks = 0; ks < 28; ++ks) vf[ks] = ldg(vsrc + 512 * ks);
#pragma unroll
    for (int ks = 0; ks < 28; ++ks) asm volatile("" : "+a"(vf[ks]));
    ring_issue(ws, 8, 0, w, lane);
    u32x4 xf[28];
#pragma unroll
    for (int ks = 0; ks < 28; ++ks) xf[ks] = lds128(2 * 28672 + lane * 16 + ks * 1024);
    f32x16 av = {};
    for (int s = 6; s < NSEG; ++s) {
        WAIT_VM(9);
        bar();
        if (s >= 8 && w == 0 && lane == 0) cnt_add(ws, F_K + (s - 2) * 4);
        const unsigned char* rsrc = ws + O_XT + (size_t)min(s + 3, NSEG - 1) * 28672 + w * 7168 + lane * 16;
        const unsigned rdst = ((s + 3) & 3) * 28672 + w * 7168, so = max(s - 1, 6) * 458752;
        const LAS u32x4* base = (const LAS u32x4*)(uintptr_t)(((s + 1) & 3) * 28672 + lane * 16);
        __builtin_amdgcn_sched_barrier(0);
        f32x16 ak = {};
        u32x4 o;
#pragma unroll
        for (int ks = 0; ks < 28; ++ks) {
            ak = mfma32(kf[ks], xf[ks], ak);
            if (ks >= 2 && ks < 10) { const int i = ks - 2; o[i & 3] = pk(av[2 * i], av[2 * i + 1]); if ((i & 3) == 3) bst4(wr, dv + (i >> 2) * 512, so, o); }
            if (ks >= 10 && ks < 24 && !(ks & 1)) glds16(rsrc + (ks - 10) / 2 * 1024, rdst + (ks - 10) / 2 * 1024);
            __builtin_amdgcn_sched_barrier(0);
        }
        av = f32x16{};
#pragma unroll
        for (int ks = 0; ks < 28; ++ks) {
            av = mfma32(xf[ks], vf[ks], av);
            if (ks) xf[ks - 1] = base[(ks - 1) * 64];
            if (ks >= 2 && ks < 10) { const int i = ks - 2; o[i & 3] = pk(ak[2 * i], ak[2 * i + 1]); if ((i & 3) == 3) bst4(wr, dk + (i >> 2) * 1024, s * 458752, o); }
            __builtin_amdgcn_sched_barrier(0);
        }
        xf[27] = base[27 * 64];
#pragma unroll
        for (int ks = 0; ks < 28; ++ks) { asm volatile("" : "+a"(kf[ks])); asm volatile("" : "+a"(vf[ks])); }
    }
    bst4(wr, dv, (NSEG - 1) * 458752, pk8(av, 0));
    bst4(wr, dv + 512, (NSEG - 1) * 458752, pk8(av, 8));
    WAIT_LGKM0();
    WAIT_VM(0);
    bar();
    if (w == 0 && lane < 2) cnt_add(ws, F_K + (NSEG - 2 + lane) * 4);
}

DEV void load_wt(u32x4 (&f)[28], const u16* src, int ld, unsigned so, int lane) {
    stage_kn(src, ld, 28, so, lane);
    WAIT_VM(0);
#pragma unroll
    for (int ks = 0; ks < 28; ++ks) f[ks] = frag_tr(so, ks, lane, true);
    WAIT_LGKM0();
#pragma unroll
    for (int ks = 0; ks < 28; ++ks) asm volatile("" : "+a"(f[ks]));
}

DEV void gluh_body(unsigned char* ws, rsrc_t wr, const u16* Wg, const u16* Wu, int ft, int s0, int s1, int w, int lane) {
    u32x4 gf[28], uf[28];
    load_wt(gf, Wg + ft * 32, FF, w * 28672, lane);
    load_wt(uf, Wu + ft * 32, FF, w * 28672, lane);
    bar();
    for (int s = 0; s < 3; ++s) ring_issue(ws, min(s0 + s, s1 - 1), s, w, lane);
    WAIT_VM(0);
    bar();
    u32x4 xf[28];
#pragma unroll
    for (int ks = 0; ks < 28; ++ks) xf[ks] = lds128(lane * 16 + ks * 1024);
    for (int s = s0; s < s1; ++s) {
        WAIT_VM(11);
        bar();
        const unsigned char* rsrc = ws + O_XT + (size_t)min(s + 3, s1 - 1) * 28672 + w * 7168 + lane * 16;
        const unsigned rdst = ((s - s0 + 3) & 3) * 28672 + w * 7168;
        const LAS u32x4* base = (const LAS u32x4*)(uintptr_t)(((s - s0 + 1) & 3) * 28672 + lane * 16);
        __builtin_amdgcn_sched_barrier(0);
        f32x16 ag = {}, au = {};
#pragma unroll
        for (int ks = 0; ks < 28; ++ks) {
            ag = mfma32(gf[ks], xf[ks], ag);
            if (ks) xf[ks - 1] = base[(ks - 1) * 64];
            au = mfma32(uf[ks], xf[ks], au);
            if (ks >= 4 && ks < 25 && (ks - 4) % 3 == 0) glds16(rsrc + (ks - 4) / 3 * 1024, rdst + (ks - 4) / 3 * 1024);
            __builtin_amdgcn_sched_barrier(0);
        }
        xf[27] = base[27 * 64];
#pragma unroll
        for (int i = 0; i < 16; ++i) ag[i] = rbf(ag[i]) * rbf(au[i]);
        const unsigned d = (unsigned)O_HT + ft * 2048 + lane * 16;
        bst4(wr, d, s * 114688, pk8(ag, 0));
        bst4(wr, d + 1024, s * 114688, pk8(ag, 8));
#pragma unroll
        for (int ks = 0; ks < 28; ++ks) { asm volatile("" : "+a"(gf[ks])); asm volatile("" : "+a"(uf[ks])); }
    }
    WAIT_LGKM0();
    WAIT_VM(0);
    bar();
    if (w == 0 && lane < s1 - s0) arrive(ws, wr, F_H + (s0 + lane) * 4, 14);
}

DEV void glu2_body(unsigned char* ws, rsrc_t wr, const u16* Wd, int cp, int s0, int s1, int w, int lane, unsigned& budget) {
    u32x4 d0[28], d1[28];
    load_wt(d0, Wd + (size_t)w * 448 * 448 + cp * 64, 448, w * 28672, lane);
    load_wt(d1, Wd + (size_t)w * 448 * 448 + cp * 64 + 32, 448, w * 28672, lane);
    bar();
    unsigned long long known = 0;
    for (int s = s0; s < s1; ++s) {
        flag_wait(wr, F_H, s, known, budget, lane, 1);
        const unsigned src = (unsigned)O_HT + w * 28672 + lane * 16;
        u32x4 hf[28];
#pragma unroll
        for (int ks = 0; ks < 28; ++ks) hf[ks] = bld4(wr, src + ks * 1024, s * 114688);
        __builtin_amdgcn_sched_barrier(0);
        f32x16 a0 = {}, a1 = {};
#pragma unroll
        for (int ks = 0; ks < 28; ++ks) { a0 = mfma32(d0[ks], hf[ks], a0); a1 = mfma32(d1[ks], hf[ks], a1); }
        const unsigned bb = (s & 1) * 32768;
#pragma unroll
        for (int q = 0; q < 4; ++q) {
            sts128f(bb + w * 8192 + q * 1024 + lane * 16, f32x4{a0[4 * q], a0[4 * q + 1], a0[4 * q + 2], a0[4 * q + 3]});
            sts128f(bb + w * 8192 + (4 + q) * 1024 + lane * 16, f32x4{a1[4 * q], a1[4 * q + 1], a1[4 * q + 2], a1[4 * q + 3]});
        }
        WAIT_LGKM0();
        bar();
        f32x4 r0 = {}, r1 = {};
#pragma unroll
        for (int ww = 0; ww < 4; ++ww) { r0 += lds128f(bb + ww * 8192 + (2 * w) * 1024 + lane * 16); r1 += lds128f(bb + ww * 8192 + (2 * w + 1) * 1024 + lane * 16); }
        u32x4 o; o.x = pk(r0[0], r0[1]); o.y = pk(r0[2], r0[3]); o.z = pk(r1[0], r1[1]); o.w = pk(r1[2], r1[3]);
        bst4(wr, (unsigned)O_GT + cp * 4096 + w * 1024 + lane * 16, s * 28672, o);
#pragma unroll
        for (int ks = 0; ks < 28; ++ks) { asm volatile("" : "+a"(d0[ks])); asm volatile("" : "+a"(d1[ks])); }
    }
    WAIT_VM(0);
    bar();
    if (w == 0 && lane < s1 - s0) arrive(ws, wr, F_G + (s0 + lane) * 4, 7);
}

DEV unsigned memoff(int i, int ch) { return (ch >> 1) * 256 + i * 32 + (ch & 1) * 16; }

#define RUNGS [c0] "s"(((xc + 1) & 3) * HO_GRP), [c1] "s"(((xc + 2) & 3) * HO_GRP), [c2] "s"(((xc + 3) & 3) * HO_GRP), [c3] "s"(xc * HO_GRP), [t0] "s"(t0)
#define OTS [ots] "v"(xc * HO_GRP + HO_TS + min(lane, 27) * 64)
DEV u32x4 onehot(int e) { const unsigned one = 0x3F80u << (16 * (e & 1)); return u32x4{(e >> 1) == 0 ? one : 0u, (e >> 1) == 1 ? one : 0u, (e >> 1) == 2 ? one : 0u, (e >> 1) == 3 ? one : 0u}; }

DEV void a_worker(unsigned char* ws, rsrc_t wr, u32x4 rw, u32x4 rh, unsigned t0, int h, int j, int w, int lane) {
    const int n = lane & 15, kg = lane >> 4, i8 = n & 7, up = n >> 3;
    const unsigned vw0 = wtoff(h * 448 + 112 * j + 8 * (n >> 2) + (n & 3), 16 * w + kg), vw1 = wtoff(h * 448 + 112 * j + 96 + n, 16 * w + kg);
    const unsigned lb = memoff(i8, 16 * w + 2 * kg + up), ob = lb, xc = j;
    const unsigned ls0 = 20480 + memoff(i8, 14 * j + 4 * up + kg), ls1 = 20480 + (up ? (7 * j + 6) * 256 + i8 * 32 + kg * 8 : memoff(i8, 14 * j + 8 + kg));
    const unsigned las = (w * 2 + up) * 256 + i8 * 32 + (kg & 1) * 16, lr0 = 2048 + w * 3712 + i8 * 464 + up * 128 + kg * 32, lr1 = 2048 + w * 3712 + i8 * 464 + (up ? 384 + kg * 16 : 256 + kg * 32);
    const u32x4 selt = onehot((i8 >> 2) == (kg & 1) ? 4 * up + (i8 & 3) : 8);
    const unsigned idx = up ? 0xEEEEEEEEu : 0x44444444u;
    if (threadIdx.x < 4) *(LAS unsigned*)(uintptr_t)(20480 + 7168 + threadIdx.x * 4) = 0;
    if (w < 3) {
        const int tid = w * 64 + lane, key = tid & 7, kc = tid >> 3;
        const unsigned lrr = 2048 + key * 464 + kc * 32, ort = (unsigned)O_RT + (h >> 2) * 28672 + (14 * j + kc) * 512 + ((h & 3) * 8 + key) * 16;
        const unsigned long long mrr = w == 0 ? ~0ull : w == 1 ? 0xFFFFFFFFFFFFull : 0ull;
        asm volatile(
        "s_mov_b32 s44, 0\n\t"
        "buffer_load_dwordx4 v[0:3], %[vw0], %[rw], s44 offen offset:0\n\t"
        "buffer_load_dwordx4 v[4:7], %[vw0], %[rw], s44 offen offset:1024\n\t"
        "buffer_load_dwordx4 v[8:11], %[vw0], %[rw], s44 offen offset:2048\n\t"
        "buffer_load_dwordx4 v[12:15], %[vw0], %[rw], s44 offen offset:3072\n\t"
        "s_mov_b32 s44, 28672\n\t"
        "buffer_load_dwordx4 v[16:19], %[vw0], %[rw], s44 offen offset:0\n\t"
        "buffer_load_dwordx4 v[20:23], %[vw0], %[rw], s44 offen offset:1024\n\t"
        "buffer_load_dwordx4 v[24:27], %[vw0], %[rw], s44 offen offset:2048\n\t"
        "buffer_load_dwordx4 v[28:31], %[vw0], %[rw], s44 offen offset:3072\n\t"
        "s_mov_b32 s44, 64\n\t"
        "buffer_load_dwordx4 v[32:35], %[vw0], %[rw], s44 offen offset:0\n\t"
        "buffer_load_dwordx4 v[36:39], %[vw0], %[rw], s44 offen offset:1024\n\t"
        "buffer_load_dwordx4 v[40:43], %[vw0], %[rw], s44 offen offset:2048\n\t"
        "buffer_load_dwordx4 v[44:47], %[vw0], %[rw], s44 offen offset:3072\n\t"
        "s_mov_b32 s44, 28736\n\t"
        "buffer_load_dwordx4 v[48:51], %[vw0], %[rw], s44 offen offset:0\n\t"
        "buffer_load_dwordx4 v[52:55], %[vw0], %[rw], s44 offen offset:1024\n\t"
        "buffer_load_dwordx4 v[56:59], %[vw0], %[rw], s44 offen offset:2048\n\t"
        "buffer_load_dwordx4 v[60:63], %[vw0], %[rw], s44 offen offset:3072\n\t"
        "s_waitcnt vmcnt(8)\n\t"
        "v_permlane16_swap_b32 v0, v2\n\t"
        "v_permlane16_swap_b32 v1, v3\n\t"
        "v_permlane16_swap_b32 v4, v6\n\t"
        "v_permlane16_swap_b32 v5, v7\n\t"
        "v_permlane16_swap_b32 v8, v10\n\t"
        "v_permlane16_swap_b32 v9, v11\n\t"
        "v_permlane16_swap_b32 v12, v14\n\t"
        "v_permlane16_swap_b32 v13, v15\n\t"
        "v_permlane16_swap_b32 v16, v18\n\t"
        "v_permlane16_swap_b32 v17, v19\n\t"
        "v_permlane16_swap_b32 v20, v22\n\t"
        "v_permlane16_swap_b32 v21, v23\n\t"
        "v_permlane16_swap_b32 v24, v26\n\t"
        "v_permlane16_swap_b32 v25, v27\n\t"
        "v_permlane16_swap_b32 v28, v30\n\t"
        "v_permlane16_swap_b32 v29, v31\n\t"
        "v_accvgpr_write_b32 a0, v0\n\t"
        "v_accvgpr_write_b32 a1, v2\n\t"
        "v_accvgpr_write_b32 a2, v1\n\t"
        "v_accvgpr_write_b32 a3, v3\n\t"
        "v_accvgpr_write_b32 a4, v4\n\t"
        "v_accvgpr_write_b32 a5, v6\n\t"
        "v_accvgpr_write_b32 a6, v5\n\t"
        "v_accvgpr_write_b32 a7, v7\n\t"
        "v_accvgpr_write_b32 a8, v8\n\t"
        "v_accvgpr_write_b32 a9, v10\n\t"
        "v_accvgpr_write_b32 a10, v9\n\t"
        "v_accvgpr_write_b32 a11, v11\n\t"
        "v_accvgpr_write_b32 a12, v12\n\t"
        "v_accvgpr_write_b32 a13, v14\n\t"
        "v_accvgpr_write_b32 a14, v13\n\t"
        "v_accvgpr_write_b32 a15, v15\n\t"
        "v_accvgpr_write_b32 a16, v16\n\t"
        "v_accvgpr_write_b32 a17, v18\n\t"
        "v_accvgpr_write_b32 a18, v17\n\t"
        "v_accvgpr_write_b32 a19, v19\n\t"
        "v_accvgpr_write_b32 a20, v20\n\t"
        "v_accvgpr_write_b32 a21, v22\n\t"
        "v_accvgpr_write_b32 a22, v21\n\t"
        "v_accvgpr_write_b32 a23, v23\n\t"
        "v_accvgpr_write_b32 a24, v24\n\t"
        "v_accvgpr_write_b32 a25, v26\n\t"
        "v_accvgpr_write_b32 a26, v25\n\t"
        "v_accvgpr_write_b32 a27, v27\n\t"
        "v_accvgpr_write_b32 a28, v28\n\t"
        "v_accvgpr_write_b32 a29, v30\n\t"
        "v_accvgpr_write_b32 a30, v29\n\t"
        "v_accvgpr_write_b32 a31, v31\n\t"
        "s_mov_b32 s44, 57344\n\t"
        "buffer_load_dwordx4 v[0:3], %[vw0], %[rw], s44 offen offset:0\n\t"
        "buffer_load_dwordx4 v[4:7], %[vw0], %[rw], s44 offen offset:1024\n\t"
        "buffer_load_dwordx4 v[8:11], %[vw0], %[rw], s44 offen offset:2048\n\t"
        "buffer_load_dwordx4 v[12:15], %[vw0], %[rw], s44 offen offset:3072\n\t"
        "s_mov_b32 s44, 0\n\t"
        "buffer_load_dwordx4 v[16:19], %[vw1], %[rw], s44 offen offset:0\n\t"
        "buffer_load_dwordx4 v[20:23], %[vw1], %[rw], s44 offen offset:1024\n\t"
        "buffer_load_dwordx4 v[24:27], %[vw1], %[rw], s44 offen offset:2048\n\t"
        "buffer_load_dwordx4 v[28:31], %[vw1], %[rw], s44 offen offset:3072\n\t"
        "s_waitcnt vmcnt(8)\n\t"
        "v_permlane16_swap_b32 v32, v34\n\t"
        "v_permlane16_swap_b32 v33, v35\n\t"
        "v_permlane16_swap_b32 v36, v38\n\t"
        "v_permlane16_swap_b32 v37, v39\n\t"
        "v_permlane16_swap_b32 v40, v42\n\t"
        "v_permlane16_swap_b32 v41, v43\n\t"
        "v_permlane16_swap_b32 v44, v46\n\t"
        "v_permlane16_swap_b32 v45, v47\n\t"
        "v_permlane16_swap_b32 v48, v50\n\t"
        "v_permlane16_swap_b32 v49, v51\n\t"
        "v_permlane16_swap_b32 v52, v54\n\t"
        "v_permlane16_swap_b32 v53, v55\n\t"
        "v_permlane16_swap_b32 v56, v58\n\t"
        "v_permlane16_swap_b32 v57, v59\n\t"
        "v_permlane16_swap_b32 v60, v62\n\t"
        "v_permlane16_swap_b32 v61, v63\n\t"
        "v_accvgpr_write_b32 a32, v32\n\t"
        "v_accvgpr_write_b32 a33, v34\n\t"
        "v_accvgpr_write_b32 a34, v33\n\t"
        "v_accvgpr_write_b32 a35, v35\n\t"
        "v_accvgpr_write_b32 a36, v36\n\t"
        "v_accvgpr_write_b32 a37, v38\n\t"
        "v_accvgpr_write_b32 a38, v37\n\t"
        "v_accvgpr_write_b32 a39, v39\n\t"
        "v_accvgpr_write_b32 a40, v40\n\t"
        "v_accvgpr_write_b32 a41, v42\n\t"
        "v_accvgpr_write_b32 a42, v41\n\t"
        "v_accvgpr_write_b32 a43, v43\n\t"
        "v_accvgpr_write_b32 a44, v44\n\t"
        "v_accvgpr_write_b32 a45, v46\n\t"
        "v_accvgpr_write_b32 a46, v45\n\t"
        "v_accvgpr_write_b32 a47, v47\n\t"
        "v_accvgpr_write_b32 a48, v48\n\t"
        "v_accvgpr_write_b32 a49, v50\n\t"
        "v_accvgpr_write_b32 a50, v49\n\t"
        "v_accvgpr_write_b32 a51, v51\n\t"
        "v_accvgpr_write_b32 a52, v52\n\t"
        "v_accvgpr_write_b32 a53, v54\n\t"
        "v_accvgpr_write_b32 a54, v53\n\t"
        "v_accvgpr_write_b32 a55, v55\n\t"
        "v_accvgpr_write_b32 a56, v56\n\t"
        "v_accvgpr_write_b32 a57, v58\n\t"
        "v_accvgpr_write_b32 a58, v57\n\t"
        "v_accvgpr_write_b32 a59, v59\n\t"
        "v_accvgpr_write_b32 a60, v60\n\t"
        "v_accvgpr_write_b32 a61, v62\n\t"
        "v_accvgpr_write_b32 a62, v61\n\t"
        "v_accvgpr_write_b32 a63, v63\n\t"
        "s_mov_b32 s44, 57408\n\t"
        "buffer_load_dwordx4 v[32:35], %[vw0], %[rw], s44 offen offset:0\n\t"
        "buffer_load_dwordx4 v[36:39], %[vw0], %[rw], s44 offen offset:1024\n\t"
        "buffer_load_dwordx4 v[40:43], %[vw0], %[rw], s44 offen offset:2048\n\t"
        "buffer_load_dwordx4 v[44:47], %[vw0], %[rw], s44 offen offset:3072\n\t"
        "s_waitcnt vmcnt(4)\n\t"
        "v_permlane16_swap_b32 v0, v2\n\t"
        "v_permlane16_swap_b32 v1, v3\n\t"
        "v_permlane16_swap_b32 v4, v6\n\t"
        "v_permlane16_swap_b32 v5, v7\n\t"
        "v_permlane16_swap_b32 v8, v10\n\t"
        "v_permlane16_swap_b32 v9, v11\n\t"
        "v_permlane16_swap_b32 v12, v14\n\t"
        "v_permlane16_swap_b32 v13, v15\n\t"
        "v_permlane16_swap_b32 v16, v18\n\t"
        "v_permlane16_swap_b32 v17, v19\n\t"
        "v_permlane16_swap_b32 v20, v22\n\t"
        "v_permlane16_swap_b32 v21, v23\n\t"
        "v_permlane16_swap_b32 v24, v26\n\t"
        "v_permlane16_swap_b32 v25, v27\n\t"
        "v_permlane16_swap_b32 v28, v30\n\t"
        "v_permlane16_swap_b32 v29, v31\n\t"
        "v_accvgpr_write_b32 a64, v0\n\t"
        "v_accvgpr_write_b32 a65, v2\n\t"
        "v_accvgpr_write_b32 a66, v1\n\t"
        "v_accvgpr_write_b32 a67, v3\n\t"
        "v_accvgpr_write_b32 a68, v4\n\t"
        "v_accvgpr_write_b32 a69, v6\n\t"
        "v_accvgpr_write_b32 a70, v5\n\t"
        "v_accvgpr_write_b32 a71, v7\n\t"
        "v_accvgpr_write_b32 a72, v8\n\t"
        "v_accvgpr_write_b32 a73, v10\n\t"
        "v_accvgpr_write_b32 a74, v9\n\t"
        "v_accvgpr_write_b32 a75, v11\n\t"
        "v_accvgpr_write_b32 a76, v12\n\t"
        "v_accvgpr_write_b32 a77, v14\n\t"
        "v_accvgpr_write_b32 a78, v13\n\t"
        "v_accvgpr_write_b32 a79, v15\n\t"
        "v_accvgpr_write_b32 a80, v16\n\t"
        "v_accvgpr_write_b32 a81, v18\n\t"
        "v_accvgpr_write_b32 a82, v17\n\t"
        "v_accvgpr_write_b32 a83, v19\n\t"
        "v_accvgpr_write_b32 a84, v20\n\t"
        "v_accvgpr_write_b32 a85, v22\n\t"
        "v_accvgpr_write_b32 a86, v21\n\t"
        "v_accvgpr_write_b32 a87, v23\n\t"
        "v_accvgpr_write_b32 a88, v24\n\t"
        "v_accvgpr_write_b32 a89, v26\n\t"
        "v_accvgpr_write_b32 a90, v25\n\t"
        "v_accvgpr_write_b32 a91, v27\n\t"
        "v_accvgpr_write_b32 a92, v28\n\t"
        "v_accvgpr_write_b32 a93, v30\n\t"
        "v_accvgpr_write_b32 a94, v29\n\t"
        "v_accvgpr_write_b32 a95, v31\n\t"
        "s_waitcnt vmcnt(0)\n\t"
        "v_permlane16_swap_b32 v32, v34\n\t"
        "v_permlane16_swap_b32 v33, v35\n\t"
        "v_permlane16_swap_b32 v36, v38\n\t"
        "v_permlane16_swap_b32 v37, v39\n\t"
        "v_permlane16_swap_b32 v40, v42\n\t"
        "v_permlane16_swap_b32 v41, v43\n\t"
        "v_permlane16_swap_b32 v44, v46\n\t"
        "v_permlane16_swap_b32 v45, v47\n\t"
        "v_accvgpr_write_b32 a96, v32\n\t"
        "v_accvgpr_write_b32 a97, v34\n\t"
        "v_accvgpr_write_b32 a98, v33\n\t"
        "v_accvgpr_write_b32 a99, v35\n\t"
        "v_accvgpr_write_b32 a100, v36\n\t"
        "v_accvgpr_write_b32 a101, v38\n\t"
        "v_accvgpr_write_b32 a102, v37\n\t"
        "v_accvgpr_write_b32 a103, v39\n\t"
        "v_accvgpr_write_b32 a104, v40\n\t"
        "v_accvgpr_write_b32 a105, v42\n\t"
        "v_accvgpr_write_b32 a106, v41\n\t"
        "v_accvgpr_write_b32 a107, v43\n\t"
        "v_accvgpr_write_b32 a108, v44\n\t"
        "v_accvgpr_write_b32 a109, v46\n\t"
        "v_accvgpr_write_b32 a110, v45\n\t"
        "v_accvgpr_write_b32 a111, v47\n\t"
        "s_mov_b32 s48, 0xff00ff\n\t"
        "s_mov_b32 s49, 0xff00ff00\n\t"
        "s_mov_b32 s50, 0xff00ff\n\t"
        "s_mov_b32 s51, 0xff00ff\n\t"
        "s_mov_b32 s40, 0\n\t"
        "s_mov_b32 s43, %[bud]\n\t"
        "s_mov_b32 s54, 0\n\t"
        "s_mov_b32 s77, %[t0]\n\t"
        "v_mbcnt_lo_u32_b32 v0, -1, 0\n\t"
        "v_mbcnt_hi_u32_b32 v0, -1, v0\n\t"
        "v_lshlrev_b32 v0, 2, v0\n\t"
        "s_mov_b32 s82, 132\n\t"
        "s_mov_b32 s83, 62\n\t"
        "LPA_%=:\n\t"
        "buffer_load_dword v2, off, %[rw], %[fr] offset:1536 sc1\n\t"
        "s_waitcnt vmcnt(0)\n\t"
        "v_cmp_eq_u32 vcc, 0, v2\n\t"
        "s_cbranch_vccz LPB_%=\n\t"
        "s_sleep 8\n\t"
        "s_sub_u32 s43, s43, 1\n\t"
        "s_cbranch_scc0 LPA_%=\n\t"
        "s_mov_b32 s43, 0\n\t"
        "LPB_%=:\n\t"
        "buffer_load_dword v2, v0, %[rw], %[fr] offen sc1\n\t"
        "s_waitcnt vmcnt(0)\n\t"
        "v_cmp_eq_u32_e64 s[80:81], 2, v2\n\t"
        "s_bitcmp1_b64 s[80:81], s40\n\t"
        "s_cselect_b32 s44, 8192, 0\n\t"
        "s_lshl_b32 s41, s40, 18\n\t"
        "s_add_u32 s41, s41, s44\n\t"
        "v_mov_b32 v58, 0\n\t"
        "v_mov_b32 v59, 0\n\t"
        "LOOP_%=:\n\t"
        "v_mov_b32 v16, 0\n\t"
        "v_mov_b32 v17, 0\n\t"
        "v_mov_b32 v18, 0\n\t"
        "v_mov_b32 v19, 0\n\t"
        "v_mov_b32 v20, 0\n\t"
        "v_mov_b32 v21, 0\n\t"
        "v_mov_b32 v22, 0\n\t"
        "v_mov_b32 v23, 0\n\t"
        "v_mov_b32 v24, 0\n\t"
        "v_mov_b32 v25, 0\n\t"
        "v_mov_b32 v26, 0\n\t"
        "v_mov_b32 v27, 0\n\t"
        "v_mov_b32 v28, 0\n\t"
        "v_mov_b32 v29, 0\n\t"
        "v_mov_b32 v30, 0\n\t"
        "v_mov_b32 v31, 0\n\t"
        "v_mov_b32 v32, 0\n\t"
        "v_mov_b32 v33, 0\n\t"
        "v_mov_b32 v34, 0\n\t"
        "v_mov_b32 v35, 0\n\t"
        "v_mov_b32 v36, 0\n\t"
        "v_mov_b32 v37, 0\n\t"
        "v_mov_b32 v38, 0\n\t"
        "v_mov_b32 v39, 0\n\t"
        "v_mov_b32 v40, 0\n\t"
        "v_mov_b32 v41, 0\n\t"
        "v_mov_b32 v42, 0\n\t"
        "v_mov_b32 v43, 0\n\t"
        "s_add_u32 s56, s41, %[c0]\n\t"
        "s_add_u32 s42, s77, 0\n\t"
        "LUa_%=:\n\t"
        "s_memrealtime s[78:79]\n\t"
        "s_waitcnt lgkmcnt(0)\n\t"
        "s_sub_u32 s78, s78, s42\n\t"
        "s_cmp_lt_i32 s78, 0\n\t"
        "s_cbranch_scc1 LUa_%=\n\t"
        "s_cmp_ge_u32 s78, 2\n\t"
        "s_cbranch_scc1 LVa_%=\n\t"
        "s_nop 7\n\t"
        "s_nop 6\n\t"
        "LVa_%=:\n\t"
        "s_bitcmp1_b32 s78, 0\n\t"
        "s_cbranch_scc1 LWa_%=\n\t"
        "s_nop 7\n\t"
        "s_nop 0\n\t"
        "LWa_%=:\n\t"
        "buffer_load_dwordx4 v[0:3], %[ob], %[rh], s56 offen sc1\n\t"
        "buffer_load_dwordx4 v[4:7], %[ob], %[rh], s56 offen offset:1024 sc1\n\t"
        "s_waitcnt vmcnt(1)\n\t"
        "v_smfmac_f32_16x16x64_bf16 v[40:43], v[0:3], a[96:103], %[idx]\n\t"
        "s_waitcnt vmcnt(0)\n\t"
        "v_max3_u32 v88, v0, v1, v2\n\t"
        "v_max3_u32 v88, v88, v3, v4\n\t"
        "v_max3_u32 v88, v88, v5, v6\n\t"
        "v_smfmac_f32_16x16x64_bf16 v[32:35], v[0:3], a[64:71], %[idx]\n\t"
        "v_max_u32 v88, v88, v7\n\t"
        "v_cmp_eq_u32 vcc, -1, v88\n\t"
        "s_cbranch_vccnz LRa_%=\n\t"
        "LGOa_%=:\n\t"
        "v_smfmac_f32_16x16x64_bf16 v[36:39], v[0:3], a[80:87], %[idx]\n\t"
        "ds_write_b128 %[lb], v[0:3] offset:20480\n\t"
        "v_smfmac_f32_16x16x64_bf16 v[40:43], v[4:7], a[104:111], %[idx]\n\t"
        "ds_write_b128 %[lb], v[4:7] offset:21504\n\t"
        "v_smfmac_f32_16x16x64_bf16 v[32:35], v[4:7], a[72:79], %[idx]\n\t"
        "v_smfmac_f32_16x16x64_bf16 v[36:39], v[4:7], a[88:95], %[idx]\n\t"
        "v_smfmac_f32_16x16x64_bf16 v[16:19], v[0:3], a[0:7], %[idx]\n\t"
        "v_smfmac_f32_16x16x64_bf16 v[20:23], v[0:3], a[16:23], %[idx]\n\t"
        "v_smfmac_f32_16x16x64_bf16 v[16:19], v[4:7], a[8:15], %[idx]\n\t"
        "s_nop 1\n\t"
        "v_cvt_pk_bf16_f32 v56, v40, v41\n\t"
        "v_cvt_pk_bf16_f32 v57, v42, v43\n\t"
        "v_smfmac_f32_16x16x64_bf16 v[20:23], v[4:7], a[24:31], %[idx]\n\t"
        "v_cvt_pk_bf16_f32 v52, v32, v33\n\t"
        "v_cvt_pk_bf16_f32 v53, v34, v35\n\t"
        "v_smfmac_f32_16x16x64_bf16 v[24:27], v[0:3], a[32:39], %[idx]\n\t"
        "v_cvt_pk_bf16_f32 v54, v36, v37\n\t"
        "v_cvt_pk_bf16_f32 v55, v38, v39\n\t"
        "v_smfmac_f32_16x16x64_bf16 v[28:31], v[0:3], a[48:55], %[idx]\n\t"
        "s_waitcnt lgkmcnt(0)\n\t"
        "s_barrier\n\t"
        "ds_read2_b64 v[12:15], %[ls1] offset0:0 offset1:1\n\t"
        "v_smfmac_f32_16x16x64_bf16 v[24:27], v[4:7], a[40:47], %[idx]\n\t"
        "ds_read_b128 v[8:11], %[ls0]\n\t"
        "v_cvt_pk_bf16_f32 v44, v16, v17\n\t"
        "v_cvt_pk_bf16_f32 v45, v18, v19\n\t"
        "v_smfmac_f32_16x16x64_bf16 v[28:31], v[4:7], a[56:63], %[idx]\n\t"
        "v_cvt_pk_bf16_f32 v46, v20, v21\n\t"
        "v_cvt_pk_bf16_f32 v47, v22, v23\n\t"
        "v_mfma_f32_16x16x32_bf16 v[72:75], v[56:59], %[selt], 0\n\t"
        "v_mfma_f32_16x16x32_bf16 v[68:71], v[52:55], %[selt], 0\n\t"
        "v_cvt_pk_bf16_f32 v48, v24, v25\n\t"
        "v_cvt_pk_bf16_f32 v49, v26, v27\n\t"
        "v_mfma_f32_16x16x32_bf16 v[60:63], v[44:47], %[selt], 0\n\t"
        "s_nop 0\n\t"
        "v_cvt_pk_bf16_f32 v50, v28, v29\n\t"
        "v_cvt_pk_bf16_f32 v51, v30, v31\n\t"
        "s_nop 0\n\t"
        "v_cvt_pk_bf16_f32 v82, v72, v73\n\t"
        "v_mfma_f32_16x16x32_bf16 v[64:67], v[48:51], %[selt], 0\n\t"
        "v_cvt_pk_bf16_f32 v83, v74, v75\n\t"
        "v_cvt_pk_bf16_f32 v80, v68, v69\n\t"
        "v_cvt_pk_bf16_f32 v81, v70, v71\n\t"
        "s_waitcnt lgkmcnt(0)\n\t"
        "s_nop 0\n\t"
        "v_mfma_f32_16x16x32_bf16 v[84:87], v[80:83], v[12:15], 0\n\t"
        "v_cvt_pk_bf16_f32 v76, v60, v61\n\t"
        "v_cvt_pk_bf16_f32 v77, v62, v63\n\t"
        "v_cvt_pk_bf16_f32 v78, v64, v65\n\t"
        "v_cvt_pk_bf16_f32 v79, v66, v67\n\t"
        "s_nop 1\n\t"
        "v_mfma_f32_16x16x32_bf16 v[84:87], v[76:79], v[8:11], v[84:87]\n\t"
        "s_mov_b64 exec, s[48:49]\n\t"
        "s_nop 6\n\t"
        "ds_write_b128 %[las], v[84:87]\n\t"
        "s_mov_b64 exec, -1\n\t"
        "s_waitcnt lgkmcnt(0)\n\t"
        "s_barrier\n\t"
        "s_sleep 2\n\t"
        "buffer_load_dword v98, %[ots], %[rh], s41 offen sc1\n\t"
        "ds_write_b128 %[lr0], v[60:63]\n\t"
        "ds_write_b128 %[lr0], v[64:67] offset:16\n\t"
        "ds_write_b128 %[lr1], v[68:71]\n\t"
        "s_mov_b64 exec, s[50:51]\n\t"
        "ds_write_b128 %[lr1], v[72:75] offset:16\n\t"
        "s_mov_b64 exec, -1\n\t"
        "s_waitcnt vmcnt(0)\n\t"
        "LTA_%=:\n\t"
        "v_max_u32_dpp v98, v98, v98 row_shr:1 row_mask:0xf bank_mask:0xf bound_ctrl:1\n\t"
        "s_nop 1\n\t"
        "v_max_u32_dpp v98, v98, v98 row_shr:2 row_mask:0xf bank_mask:0xf bound_ctrl:1\n\t"
        "s_nop 1\n\t"
        "v_max_u32_dpp v98, v98, v98 row_shr:4 row_mask:0xf bank_mask:0xf bound_ctrl:1\n\t"
        "s_nop 1\n\t"
        "v_max_u32_dpp v98, v98, v98 row_shr:8 row_mask:0xf bank_mask:0xf bound_ctrl:1\n\t"
        "s_nop 0\n\t"
        "v_readlane_b32 s44, v98, 15\n\t"
        "v_readlane_b32 s42, v98, 31\n\t"
        "s_nop 1\n\t"
        "s_max_u32 s44, s44, s42\n\t"
        "s_cmp_eq_u32 s44, -1\n\t"
        "s_cbranch_scc1 LTS_%=\n\t"
        "s_add_u32 s77, s77, s44\n\t"
        "s_cmp_ge_u32 s40, 6\n\t"
        "s_cselect_b32 s42, s44, 0\n\t"
        "s_cmp_lg_u32 s42, 0\n\t"
        "s_cselect_b32 s42, 2, 0\n\t"
        "s_add_u32 s82, s82, s42\n\t"
        "s_min_u32 s82, s82, 136\n\t"
        "LTB_%=:\n\t"
        "s_add_u32 s77, s77, s82\n\t"
        "s_cmp_lt_u32 s40, %[pmin]\n\t"
        "s_cbranch_scc1 LNP_%=\n\t"
        "s_lshl_b32 s44, s40, 2\n\t"
        "s_add_u32 s44, s44, %[fp]\n\t"
        "s_mov_b64 exec, 1\n\t"
        "v_mov_b32 v88, 1\n\t"
        "v_mov_b32 v89, 0\n\t"
        "buffer_store_dword v88, v89, %[rw], s44 offen sc1\n\t"
        "LNP_%=:\n\t"
        "s_mov_b64 exec, -1\n\t"
        "s_waitcnt lgkmcnt(0)\n\t"
        "s_barrier\n\t"
        "s_mov_b64 exec, %[mrr]\n\t"
        "s_cbranch_execz LNR_%=\n\t"
        "ds_read_b128 v[0:3], %[lrr] offset:0\n\t"
        "ds_read_b128 v[4:7], %[lrr] offset:16\n\t"
        "ds_read_b128 v[8:11], %[lrr] offset:3712\n\t"
        "ds_read_b128 v[12:15], %[lrr] offset:3728\n\t"
        "ds_read_b128 v[16:19], %[lrr] offset:7424\n\t"
        "ds_read_b128 v[20:23], %[lrr] offset:7440\n\t"
        "ds_read_b128 v[24:27], %[lrr] offset:11136\n\t"
        "ds_read_b128 v[28:31], %[lrr] offset:11152\n\t"
        "s_waitcnt lgkmcnt(0)\n\t"
        "v_pk_add_f32 v[0:1], v[0:1], v[8:9]\n\t"
        "v_pk_add_f32 v[16:17], v[16:17], v[24:25]\n\t"
        "v_pk_add_f32 v[0:1], v[0:1], v[16:17]\n\t"
        "v_pk_add_f32 v[2:3], v[2:3], v[10:11]\n\t"
        "v_pk_add_f32 v[18:19], v[18:19], v[26:27]\n\t"
        "v_pk_add_f32 v[2:3], v[2:3], v[18:19]\n\t"
        "v_pk_add_f32 v[4:5], v[4:5], v[12:13]\n\t"
        "v_pk_add_f32 v[20:21], v[20:21], v[28:29]\n\t"
        "v_pk_add_f32 v[4:5], v[4:5], v[20:21]\n\t"
        "v_pk_add_f32 v[6:7], v[6:7], v[14:15]\n\t"
        "v_pk_add_f32 v[22:23], v[22:23], v[30:31]\n\t"
        "v_pk_add_f32 v[6:7], v[6:7], v[22:23]\n\t"
        "v_cvt_pk_bf16_f32 v88, v0, v1\n\t"
        "v_cvt_pk_bf16_f32 v89, v2, v3\n\t"
        "v_cvt_pk_bf16_f32 v90, v4, v5\n\t"
        "v_cvt_pk_bf16_f32 v91, v6, v7\n\t"
        "buffer_store_dwordx4 v[88:91], %[ort], %[rw], s54 offen sc1\n\t"
        "LNR_%=:\n\t"
        "s_mov_b64 exec, -1\n\t"
        "s_add_u32 s40, s40, 1\n\t"
        "s_add_u32 s54, s54, 114688\n\t"
        "s_bitcmp1_b64 s[80:81], s40\n\t"
        "s_cselect_b32 s44, 8192, 0\n\t"
        "s_lshl_b32 s41, s40, 18\n\t"
        "s_add_u32 s41, s41, s44\n\t"
        "s_cmp_lt_u32 s40, 64\n\t"
        "s_cbranch_scc1 LOOP_%=\n\t"
        "s_waitcnt vmcnt(0)\n\t"
        "s_branch LOUT_%=\n\t"
        "LRa_%=:\n\t"
        "s_mov_b32 s59, %[c1]\n\t"
        "s_mov_b32 s58, %[c2]\n\t"
        "LPa_%=:\n\t"
        "s_mov_b32 s57, s58\n\t"
        "s_mov_b32 s58, %[c3]\n\t"
        "s_add_u32 s56, s41, s59\n\t"
        "s_mov_b32 s59, s57\n\t"
        "buffer_load_dwordx4 v[0:3], %[ob], %[rh], s56 offen sc1\n\t"
        "buffer_load_dwordx4 v[4:7], %[ob], %[rh], s56 offen offset:1024 sc1\n\t"
        "s_waitcnt vmcnt(0)\n\t"
        "v_max3_u32 v88, v0, v1, v2\n\t"
        "v_max3_u32 v88, v88, v3, v4\n\t"
        "v_max3_u32 v88, v88, v5, v6\n\t"
        "v_max_u32 v88, v88, v7\n\t"
        "v_cmp_eq_u32 vcc, -1, v88\n\t"
        "s_cbranch_vccz LKa_%=\n\t"
        "s_sub_u32 s43, s43, 1\n\t"
        "s_cbranch_scc0 LPa_%=\n\t"
        "s_mov_b32 s43, 0\n\t"
        "v_mov_b32 v0, 0\n\t"
        "v_mov_b32 v1, 0\n\t"
        "v_mov_b32 v2, 0\n\t"
        "v_mov_b32 v3, 0\n\t"
        "v_mov_b32 v4, 0\n\t"
        "v_mov_b32 v5, 0\n\t"
        "v_mov_b32 v6, 0\n\t"
        "v_mov_b32 v7, 0\n\t"
        "LKa_%=:\n\t"
        "v_mov_b32 v32, 0\n\t"
        "v_mov_b32 v33, 0\n\t"
        "v_mov_b32 v34, 0\n\t"
        "v_mov_b32 v35, 0\n\t"
        "v_mov_b32 v40, 0\n\t"
        "v_mov_b32 v41, 0\n\t"
        "v_mov_b32 v42, 0\n\t"
        "v_mov_b32 v43, 0\n\t"
        "s_nop 1\n\t"
        "v_smfmac_f32_16x16x64_bf16 v[40:43], v[0:3], a[96:103], %[idx]\n\t"
        "v_smfmac_f32_16x16x64_bf16 v[32:35], v[0:3], a[64:71], %[idx]\n\t"
        "s_branch LGOa_%=\n\t"
        "LTS_%=:\n\t"
        "buffer_load_dword v98, %[ots], %[rh], s41 offen sc1\n\t"
        "s_waitcnt vmcnt(0)\n\t"
        "s_sub_u32 s43, s43, 1\n\t"
        "s_cbranch_scc0 LTA_%=\n\t"
        "s_mov_b32 s43, 0\n\t"
        "s_branch LTB_%=\n\t"
        "LOUT_%=:\n\t"
        :: VDBG [rw] "s"(rw), [rh] "s"(rh), [fr] "s"((unsigned)O_FLG + F_R), [bud] "s"(SPIN_LIMIT), RUNGS, OTS, [vw0] "v"(vw0), [vw1] "v"(vw1), [selt] "v"(selt), [idx] "v"(idx),
           [ob] "v"(ob), [lb] "v"(lb), [ls0] "v"(ls0), [ls1] "v"(ls1), [las] "v"(las), [lr0] "v"(lr0), [lr1] "v"(lr1), [lrr] "v"(lrr), [ort] "v"(ort), [mrr] "s"(mrr),
           [pmin] "s"((w == 2 && h == 0 && j == 0) ? 2 : 1000), [fp] "s"((unsigned)O_FLG + F_P - 8)
        :
          "v0", "v1", "v2", "v3", "v4", "v5", "v6", "v7", "v8", "v9", "v10", "v11", "v12", "v13", "v14", "v15", "v16", "v17", "v18", "v19", "v20", "v21", "v22", "v23",
          "v24", "v25", "v26", "v27", "v28", "v29", "v30", "v31", "v32", "v33", "v34", "v35", "v36", "v37", "v38", "v39", "v40", "v41", "v42", "v43", "v44", "v45", "v46", "v47",
          "v48", "v49", "v50", "v51", "v52", "v53", "v54", "v55", "v56", "v57", "v58", "v59", "v60", "v61", "v62", "v63", "v64", "v65", "v66", "v67", "v68", "v69", "v70", "v71",
          "v72", "v73", "v74", "v75", "v76", "v77", "v78", "v79", "v80", "v81", "v82", "v83", "v84", "v85", "v86", "v87", "v88", "v89", "v90", "v91", "v92", "v93", "v94", "v95",
          "v96", "v97", "v98", "a0", "a1", "a2", "a3", "a4", "a5", "a6", "a7", "a8", "a9", "a10", "a11", "a12", "a13", "a14", "a15", "a16", "a17", "a18", "a19", "a20",
          "a21", "a22", "a23", "a24", "a25", "a26", "a27", "a28", "a29", "a30", "a31", "a32", "a33", "a34", "a35", "a36", "a37", "a38", "a39", "a40", "a41", "a42", "a43", "a44",
          "a45", "a46", "a47", "a48", "a49", "a50", "a51", "a52", "a53", "a54", "a55", "a56", "a57", "a58", "a59", "a60", "a61", "a62", "a63", "a64", "a65", "a66", "a67", "a68",
          "a69", "a70", "a71", "a72", "a73", "a74", "a75", "a76", "a77", "a78", "a79", "a80", "a81", "a82", "a83", "a84", "a85", "a86", "a87", "a88", "a89", "a90", "a91", "a92",
          "a93", "a94", "a95", "a96", "a97", "a98", "a99", "a100", "a101", "a102", "a103", "a104", "a105", "a106", "a107", "a108", "a109", "a110", "a111", "s40", "s41", "s42", "s43", "s44",
          "s45", "s46", "s47", "s48", "s49", "s50", "s51", "s52", "s53", "s54", "s55", "s56", "s57", "s58", "s59", "s60", "s61", "s62", "s63", "s64", "s65", "s66", "s67", "s68",
          "s69", "s70", "s71", "s72", "s73", "s74", "s75", "s76", "s77", "s78", "s79", "s80", "s81", "s82", "s83", "vcc", "scc", "memory");
    } else {
        const unsigned osp = (lane >> 5) * HO_GRP + HO_SP + (h * 4 + j) * 128 + (lane & 31) * 4;
        asm volatile(
        "s_mov_b32 s44, 0\n\t"
        "buffer_load_dwordx4 v[0:3], %[vw0], %[rw], s44 offen offset:0\n\t"
        "buffer_load_dwordx4 v[4:7], %[vw0], %[rw], s44 offen offset:1024\n\t"
        "s_mov_b32 s44, 28672\n\t"
        "buffer_load_dwordx4 v[8:11], %[vw0], %[rw], s44 offen offset:0\n\t"
        "buffer_load_dwordx4 v[12:15], %[vw0], %[rw], s44 offen offset:1024\n\t"
        "s_mov_b32 s44, 64\n\t"
        "buffer_load_dwordx4 v[16:19], %[vw0], %[rw], s44 offen offset:0\n\t"
        "buffer_load_dwordx4 v[20:23], %[vw0], %[rw], s44 offen offset:1024\n\t"
        "s_mov_b32 s44, 28736\n\t"
        "buffer_load_dwordx4 v[24:27], %[vw0], %[rw], s44 offen offset:0\n\t"
        "buffer_load_dwordx4 v[28:31], %[vw0], %[rw], s44 offen offset:1024\n\t"
        "s_waitcnt vmcnt(4)\n\t"
        "v_permlane16_swap_b32 v0, v2\n\t"
        "v_permlane16_swap_b32 v1, v3\n\t"
        "v_permlane16_swap_b32 v4, v6\n\t"
        "v_permlane16_swap_b32 v5, v7\n\t"
        "v_permlane16_swap_b32 v8, v10\n\t"
        "v_permlane16_swap_b32 v9, v11\n\t"
        "v_permlane16_swap_b32 v12, v14\n\t"
        "v_permlane16_swap_b32 v13, v15\n\t"
        "v_accvgpr_write_b32 a0, v0\n\t"
        "v_accvgpr_write_b32 a1, v2\n\t"
        "v_accvgpr_write_b32 a2, v1\n\t"
        "v_accvgpr_write_b32 a3, v3\n\t"
        "v_accvgpr_write_b32 a4, v4\n\t"
        "v_accvgpr_write_b32 a5, v6\n\t"
        "v_accvgpr_write_b32 a6, v5\n\t"
        "v_accvgpr_write_b32 a7, v7\n\t"
        "v_accvgpr_write_b32 a8, v8\n\t"
        "v_accvgpr_write_b32 a9, v10\n\t"
        "v_accvgpr_write_b32 a10, v9\n\t"
        "v_accvgpr_write_b32 a11, v11\n\t"
        "v_accvgpr_write_b32 a12, v12\n\t"
        "v_accvgpr_write_b32 a13, v14\n\t"
        "v_accvgpr_write_b32 a14, v13\n\t"
        "v_accvgpr_write_b32 a15, v15\n\t"
        "s_mov_b32 s44, 57344\n\t"
        "buffer_load_dwordx4 v[0:3], %[vw0], %[rw], s44 offen offset:0\n\t"
        "buffer_load_dwordx4 v[4:7], %[vw0], %[rw], s44 offen offset:1024\n\t"
        "s_mov_b32 s44, 0\n\t"
        "buffer_load_dwordx4 v[8:11], %[vw1], %[rw], s44 offen offset:0\n\t"
        "buffer_load_dwordx4 v[12:15], %[vw1], %[rw], s44 offen offset:1024\n\t"
        "s_waitcnt vmcnt(4)\n\t"
        "v_permlane16_swap_b32 v16, v18\n\t"
        "v_permlane16_swap_b32 v17, v19\n\t"
        "v_permlane16_swap_b32 v20, v22\n\t"
        "v_permlane16_swap_b32 v21, v23\n\t"
        "v_permlane16_swap_b32 v24, v26\n\t"
        "v_permlane16_swap_b32 v25, v27\n\t"
        "v_permlane16_swap_b32 v28, v30\n\t"
        "v_permlane16_swap_b32 v29, v31\n\t"
        "v_accvgpr_write_b32 a16, v16\n\t"
        "v_accvgpr_write_b32 a17, v18\n\t"
        "v_accvgpr_write_b32 a18, v17\n\t"
        "v_accvgpr_write_b32 a19, v19\n\t"
        "v_accvgpr_write_b32 a20, v20\n\t"
        "v_accvgpr_write_b32 a21, v22\n\t"
        "v_accvgpr_write_b32 a22, v21\n\t"
        "v_accvgpr_write_b32 a23, v23\n\t"
        "v_accvgpr_write_b32 a24, v24\n\t"
        "v_accvgpr_write_b32 a25, v26\n\t"
        "v_accvgpr_write_b32 a26, v25\n\t"
        "v_accvgpr_write_b32 a27, v27\n\t"
        "v_accvgpr_write_b32 a28, v28\n\t"
        "v_accvgpr_write_b32 a29, v30\n\t"
        "v_accvgpr_write_b32 a30, v29\n\t"
        "v_accvgpr_write_b32 a31, v31\n\t"
        "s_mov_b32 s44, 57408\n\t"
        "buffer_load_dwordx4 v[16:19], %[vw0], %[rw], s44 offen offset:0\n\t"
        "buffer_load_dwordx4 v[20:23], %[vw0], %[rw], s44 offen offset:1024\n\t"
        "s_waitcnt vmcnt(2)\n\t"
        "v_permlane16_swap_b32 v0, v2\n\t"
        "v_permlane16_swap_b32 v1, v3\n\t"
        "v_permlane16_swap_b32 v4, v6\n\t"
        "v_permlane16_swap_b32 v5, v7\n\t"
        "v_permlane16_swap_b32 v8, v10\n\t"
        "v_permlane16_swap_b32 v9, v11\n\t"
        "v_permlane16_swap_b32 v12, v14\n\t"
        "v_permlane16_swap_b32 v13, v15\n\t"
        "v_accvgpr_write_b32 a32, v0\n\t"
        "v_accvgpr_write_b32 a33, v2\n\t"
        "v_accvgpr_write_b32 a34, v1\n\t"
        "v_accvgpr_write_b32 a35, v3\n\t"
        "v_accvgpr_write_b32 a36, v4\n\t"
        "v_accvgpr_write_b32 a37, v6\n\t"
        "v_accvgpr_write_b32 a38, v5\n\t"
        "v_accvgpr_write_b32 a39, v7\n\t"
        "v_accvgpr_write_b32 a40, v8\n\t"
        "v_accvgpr_write_b32 a41, v10\n\t"
        "v_accvgpr_write_b32 a42, v9\n\t"
        "v_accvgpr_write_b32 a43, v11\n\t"
        "v_accvgpr_write_b32 a44, v12\n\t"
        "v_accvgpr_write_b32 a45, v14\n\t"
        "v_accvgpr_write_b32 a46, v13\n\t"
        "v_accvgpr_write_b32 a47, v15\n\t"
        "s_waitcnt vmcnt(0)\n\t"
        "v_permlane16_swap_b32 v16, v18\n\t"
        "v_permlane16_swap_b32 v17, v19\n\t"
        "v_permlane16_swap_b32 v20, v22\n\t"
        "v_permlane16_swap_b32 v21, v23\n\t"
        "v_accvgpr_write_b32 a48, v16\n\t"
        "v_accvgpr_write_b32 a49, v18\n\t"
        "v_accvgpr_write_b32 a50, v17\n\t"
        "v_accvgpr_write_b32 a51, v19\n\t"
        "v_accvgpr_write_b32 a52, v20\n\t"
        "v_accvgpr_write_b32 a53, v22\n\t"
        "v_accvgpr_write_b32 a54, v21\n\t"
        "v_accvgpr_write_b32 a55, v23\n\t"
        "s_mov_b32 s48, 0xff00ff\n\t"
        "s_mov_b32 s49, 0xff00ff00\n\t"
        "s_mov_b32 s50, 0xff00ff\n\t"
        "s_mov_b32 s51, 0xff00ff\n\t"
        "s_mov_b32 s40, 0\n\t"
        "s_mov_b32 s43, %[bud]\n\t"
        "s_mov_b32 s54, 0\n\t"
        "s_mov_b32 s77, %[t0]\n\t"
        "v_mbcnt_lo_u32_b32 v0, -1, 0\n\t"
        "v_mbcnt_hi_u32_b32 v0, -1, v0\n\t"
        "v_lshlrev_b32 v0, 2, v0\n\t"
        "s_mov_b32 s82, 132\n\t"
        "s_mov_b32 s83, 62\n\t"
        "LPA_%=:\n\t"
        "buffer_load_dword v2, off, %[rw], %[fr] offset:1536 sc1\n\t"
        "s_waitcnt vmcnt(0)\n\t"
        "v_cmp_eq_u32 vcc, 0, v2\n\t"
        "s_cbranch_vccz LPB_%=\n\t"
        "s_sleep 8\n\t"
        "s_sub_u32 s43, s43, 1\n\t"
        "s_cbranch_scc0 LPA_%=\n\t"
        "s_mov_b32 s43, 0\n\t"
        "LPB_%=:\n\t"
        "buffer_load_dword v2, v0, %[rw], %[fr] offen sc1\n\t"
        "s_waitcnt vmcnt(0)\n\t"
        "v_cmp_eq_u32_e64 s[80:81], 2, v2\n\t"
        "s_bitcmp1_b64 s[80:81], s40\n\t"
        "s_cselect_b32 s44, 8192, 0\n\t"
        "s_lshl_b32 s41, s40, 18\n\t"
        "s_add_u32 s41, s41, s44\n\t"
        "v_mov_b32 v58, 0\n\t"
        "v_mov_b32 v59, 0\n\t"
        "LOOP_%=:\n\t"
        "v_mov_b32 v16, 0\n\t"
        "v_mov_b32 v17, 0\n\t"
        "v_mov_b32 v18, 0\n\t"
        "v_mov_b32 v19, 0\n\t"
        "v_mov_b32 v20, 0\n\t"
        "v_mov_b32 v21, 0\n\t"
        "v_mov_b32 v22, 0\n\t"
        "v_mov_b32 v23, 0\n\t"
        "v_mov_b32 v24, 0\n\t"
        "v_mov_b32 v25, 0\n\t"
        "v_mov_b32 v26, 0\n\t"
        "v_mov_b32 v27, 0\n\t"
        "v_mov_b32 v28, 0\n\t"
        "v_mov_b32 v29, 0\n\t"
        "v_mov_b32 v30, 0\n\t"
        "v_mov_b32 v31, 0\n\t"
        "v_mov_b32 v32, 0\n\t"
        "v_mov_b32 v33, 0\n\t"
        "v_mov_b32 v34, 0\n\t"
        "v_mov_b32 v35, 0\n\t"
        "v_mov_b32 v36, 0\n\t"
        "v_mov_b32 v37, 0\n\t"
        "v_mov_b32 v38, 0\n\t"
        "v_mov_b32 v39, 0\n\t"
        "v_mov_b32 v40, 0\n\t"
        "v_mov_b32 v41, 0\n\t"
        "v_mov_b32 v42, 0\n\t"
        "v_mov_b32 v43, 0\n\t"
        "s_add_u32 s56, s41, %[c0]\n\t"
        "s_add_u32 s42, s77, 0\n\t"
        "LUa_%=:\n\t"
        "s_memrealtime s[78:79]\n\t"
        "s_waitcnt lgkmcnt(0)\n\t"
        "s_sub_u32 s78, s78, s42\n\t"
        "s_cmp_lt_i32 s78, 0\n\t"
        "s_cbranch_scc1 LUa_%=\n\t"
        "s_cmp_ge_u32 s78, 2\n\t"
        "s_cbranch_scc1 LVa_%=\n\t"
        "s_nop 7\n\t"
        "s_nop 6\n\t"
        "LVa_%=:\n\t"
        "s_bitcmp1_b32 s78, 0\n\t"
        "s_cbranch_scc1 LWa_%=\n\t"
        "s_nop 7\n\t"
        "s_nop 0\n\t"
        "LWa_%=:\n\t"
        "buffer_load_dwordx4 v[0:3], %[ob], %[rh], s56 offen sc1\n\t"
        "s_waitcnt vmcnt(0)\n\t"
        "v_smfmac_f32_16x16x64_bf16 v[40:43], v[0:3], a[48:55], %[idx]\n\t"
        "v_max3_u32 v88, v0, v1, v2\n\t"
        "v_max_u32 v88, v88, v3\n\t"
        "v_cmp_eq_u32 vcc, -1, v88\n\t"
        "v_smfmac_f32_16x16x64_bf16 v[32:35], v[0:3], a[32:39], %[idx]\n\t"
        "s_cbranch_vccnz LRa_%=\n\t"
        "LGOa_%=:\n\t"
        "v_smfmac_f32_16x16x64_bf16 v[36:39], v[0:3], a[40:47], %[idx]\n\t"
        "ds_write_b128 %[lb], v[0:3] offset:20480\n\t"
        "v_smfmac_f32_16x16x64_bf16 v[16:19], v[0:3], a[0:7], %[idx]\n\t"
        "v_smfmac_f32_16x16x64_bf16 v[20:23], v[0:3], a[8:15], %[idx]\n\t"
        "v_smfmac_f32_16x16x64_bf16 v[24:27], v[0:3], a[16:23], %[idx]\n\t"
        "v_smfmac_f32_16x16x64_bf16 v[28:31], v[0:3], a[24:31], %[idx]\n\t"
        "v_cvt_pk_bf16_f32 v56, v40, v41\n\t"
        "v_cvt_pk_bf16_f32 v57, v42, v43\n\t"
        "v_cvt_pk_bf16_f32 v52, v32, v33\n\t"
        "v_cvt_pk_bf16_f32 v53, v34, v35\n\t"
        "v_cvt_pk_bf16_f32 v54, v36, v37\n\t"
        "v_cvt_pk_bf16_f32 v55, v38, v39\n\t"
        "v_mfma_f32_16x16x32_bf16 v[72:75], v[56:59], %[selt], 0\n\t"
        "v_cvt_pk_bf16_f32 v44, v16, v17\n\t"
        "v_cvt_pk_bf16_f32 v45, v18, v19\n\t"
        "v_mfma_f32_16x16x32_bf16 v[68:71], v[52:55], %[selt], 0\n\t"
        "v_cvt_pk_bf16_f32 v46, v20, v21\n\t"
        "v_cvt_pk_bf16_f32 v47, v22, v23\n\t"
        "v_cvt_pk_bf16_f32 v48, v24, v25\n\t"
        "v_cvt_pk_bf16_f32 v49, v26, v27\n\t"
        "v_mfma_f32_16x16x32_bf16 v[60:63], v[44:47], %[selt], 0\n\t"
        "s_waitcnt lgkmcnt(0)\n\t"
        "s_barrier\n\t"
        "ds_read2_b64 v[12:15], %[ls1] offset0:0 offset1:1\n\t"
        "ds_read_b128 v[8:11], %[ls0]\n\t"
        "v_cvt_pk_bf16_f32 v50, v28, v29\n\t"
        "v_cvt_pk_bf16_f32 v51, v30, v31\n\t"
        "v_cvt_pk_bf16_f32 v82, v72, v73\n\t"
        "v_cvt_pk_bf16_f32 v83, v74, v75\n\t"
        "v_mfma_f32_16x16x32_bf16 v[64:67], v[48:51], %[selt], 0\n\t"
        "v_cvt_pk_bf16_f32 v80, v68, v69\n\t"
        "v_cvt_pk_bf16_f32 v81, v70, v71\n\t"
        "v_cvt_pk_bf16_f32 v76, v60, v61\n\t"
        "v_cvt_pk_bf16_f32 v77, v62, v63\n\t"
        "s_waitcnt lgkmcnt(1)\n\t"
        "v_mfma_f32_16x16x32_bf16 v[84:87], v[80:83], v[12:15], 0\n\t"
        "s_nop 1\n\t"
        "v_cvt_pk_bf16_f32 v78, v64, v65\n\t"
        "v_cvt_pk_bf16_f32 v79, v66, v67\n\t"
        "s_waitcnt lgkmcnt(0)\n\t"
        "s_nop 0\n\t"
        "v_mfma_f32_16x16x32_bf16 v[84:87], v[76:79], v[8:11], v[84:87]\n\t"
        "s_mov_b64 exec, s[48:49]\n\t"
        "s_nop 6\n\t"
        "ds_write_b128 %[las], v[84:87]\n\t"
        "s_mov_b64 exec, -1\n\t"
        "s_waitcnt lgkmcnt(0)\n\t"
        "s_barrier\n\t"
        "ds_read2_b64 v[88:91], %[l8] offset0:0 offset1:32\n\t"
        "ds_read2_b64 v[92:95], %[l8] offset0:64 offset1:96\n\t"
        "s_waitcnt lgkmcnt(1)\n\t"
        "v_pk_add_f32 v[88:89], v[88:89], v[90:91]\n\t"
        "s_waitcnt lgkmcnt(0)\n\t"
        "v_pk_add_f32 v[92:93], v[92:93], v[94:95]\n\t"
        "v_pk_add_f32 v[90:91], v[88:89], v[92:93]\n\t"
        "v_pk_add_f32 v[88:89], v[88:89], v[92:93]\n\t"
        "s_nop 1\n\t"
        "v_permlane32_swap_b32 v88, v90\n\t"
        "v_permlane32_swap_b32 v89, v91\n\t"
        "v_pk_add_f32 v[88:89], v[88:89], v[90:91]\n\t"
        "v_cvt_pk_bf16_f32 v88, v88, v89\n\t"
        "v_min_u32 v88, -2, v88\n\t"
        "buffer_store_dword v88, %[osp0], %[rh], s41 offen sc1\n\t"
        "buffer_store_dword v88, %[osp1], %[rh], s41 offen sc1\n\t"
        "buffer_load_dword v98, %[ots], %[rh], s41 offen sc1\n\t"
        "ds_write_b128 %[lr0], v[60:63]\n\t"
        "ds_write_b128 %[lr0], v[64:67] offset:16\n\t"
        "ds_write_b128 %[lr1], v[68:71]\n\t"
        "s_mov_b64 exec, s[50:51]\n\t"
        "ds_write_b128 %[lr1], v[72:75] offset:16\n\t"
        "s_mov_b64 exec, -1\n\t"
        "s_waitcnt vmcnt(0)\n\t"
        "LTA_%=:\n\t"
        "v_max_u32_dpp v98, v98, v98 row_shr:1 row_mask:0xf bank_mask:0xf bound_ctrl:1\n\t"
        "s_nop 1\n\t"
        "v_max_u32_dpp v98, v98, v98 row_shr:2 row_mask:0xf bank_mask:0xf bound_ctrl:1\n\t"
        "s_nop 1\n\t"
        "v_max_u32_dpp v98, v98, v98 row_shr:4 row_mask:0xf bank_mask:0xf bound_ctrl:1\n\t"
        "s_nop 1\n\t"
        "v_max_u32_dpp v98, v98, v98 row_shr:8 row_mask:0xf bank_mask:0xf bound_ctrl:1\n\t"
        "s_nop 0\n\t"
        "v_readlane_b32 s44, v98, 15\n\t"
        "v_readlane_b32 s42, v98, 31\n\t"
        "s_nop 1\n\t"
        "s_max_u32 s44, s44, s42\n\t"
        "s_cmp_eq_u32 s44, -1\n\t"
        "s_cbranch_scc1 LTS_%=\n\t"
        "s_add_u32 s77, s77, s44\n\t"
        "s_cmp_ge_u32 s40, 6\n\t"
        "s_cselect_b32 s42, s44, 0\n\t"
        "s_cmp_lg_u32 s42, 0\n\t"
        "s_cselect_b32 s42, 2, 0\n\t"
        "s_add_u32 s82, s82, s42\n\t"
        "s_min_u32 s82, s82, 136\n\t"
        "LTB_%=:\n\t"
        "s_add_u32 s77, s77, s82\n\t"
        "s_mov_b64 exec, -1\n\t"
        "s_waitcnt lgkmcnt(0)\n\t"
        "s_barrier\n\t"
        "s_add_u32 s40, s40, 1\n\t"
        "s_add_u32 s54, s54, 114688\n\t"
        "s_bitcmp1_b64 s[80:81], s40\n\t"
        "s_cselect_b32 s44, 8192, 0\n\t"
        "s_lshl_b32 s41, s40, 18\n\t"
        "s_add_u32 s41, s41, s44\n\t"
        "s_cmp_lt_u32 s40, 64\n\t"
        "s_cbranch_scc1 LOOP_%=\n\t"
        "s_waitcnt vmcnt(0)\n\t"
        "s_branch LOUT_%=\n\t"
        "LRa_%=:\n\t"
        "s_mov_b32 s59, %[c1]\n\t"
        "s_mov_b32 s58, %[c2]\n\t"
        "LPa_%=:\n\t"
        "s_mov_b32 s57, s58\n\t"
        "s_mov_b32 s58, %[c3]\n\t"
        "s_add_u32 s56, s41, s59\n\t"
        "s_mov_b32 s59, s57\n\t"
        "buffer_load_dwordx4 v[0:3], %[ob], %[rh], s56 offen sc1\n\t"
        "s_waitcnt vmcnt(0)\n\t"
        "v_max3_u32 v88, v0, v1, v2\n\t"
        "v_max_u32 v88, v88, v3\n\t"
        "v_cmp_eq_u32 vcc, -1, v88\n\t"
        "s_cbranch_vccz LKa_%=\n\t"
        "s_sub_u32 s43, s43, 1\n\t"
        "s_cbranch_scc0 LPa_%=\n\t"
        "s_mov_b32 s43, 0\n\t"
        "v_mov_b32 v0, 0\n\t"
        "v_mov_b32 v1, 0\n\t"
        "v_mov_b32 v2, 0\n\t"
        "v_mov_b32 v3, 0\n\t"
        "LKa_%=:\n\t"
        "v_mov_b32 v32, 0\n\t"
        "v_mov_b32 v33, 0\n\t"
        "v_mov_b32 v34, 0\n\t"
        "v_mov_b32 v35, 0\n\t"
        "v_mov_b32 v40, 0\n\t"
        "v_mov_b32 v41, 0\n\t"
        "v_mov_b32 v42, 0\n\t"
        "v_mov_b32 v43, 0\n\t"
        "s_nop 1\n\t"
        "v_smfmac_f32_16x16x64_bf16 v[40:43], v[0:3], a[48:55], %[idx]\n\t"
        "v_smfmac_f32_16x16x64_bf16 v[32:35], v[0:3], a[32:39], %[idx]\n\t"
        "s_branch LGOa_%=\n\t"
        "LTS_%=:\n\t"
        "buffer_load_dword v98, %[ots], %[rh], s41 offen sc1\n\t"
        "s_waitcnt vmcnt(0)\n\t"
        "s_sub_u32 s43, s43, 1\n\t"
        "s_cbranch_scc0 LTA_%=\n\t"
        "s_mov_b32 s43, 0\n\t"
        "s_branch LTB_%=\n\t"
        "LOUT_%=:\n\t"
        :: VDBG [rw] "s"(rw), [rh] "s"(rh), [fr] "s"((unsigned)O_FLG + F_R), [bud] "s"(SPIN_LIMIT), RUNGS, OTS, [vw0] "v"(vw0), [vw1] "v"(vw1), [selt] "v"(selt), [idx] "v"(idx),
           [ob] "v"(ob), [lb] "v"(lb), [ls0] "v"(ls0), [ls1] "v"(ls1), [las] "v"(las), [lr0] "v"(lr0), [lr1] "v"(lr1), [l8] "v"((lane & 31) * 8 + (lane >> 5) * 1024), [osp0] "v"(osp), [osp1] "v"(osp + 2 * HO_GRP)
        :
          "v0", "v1", "v2", "v3", "v4", "v5", "v6", "v7", "v8", "v9", "v10", "v11", "v12", "v13", "v14", "v15", "v16", "v17", "v18", "v19", "v20", "v21", "v22", "v23",
          "v24", "v25", "v26", "v27", "v28", "v29", "v30", "v31", "v32", "v33", "v34", "v35", "v36", "v37", "v38", "v39", "v40", "v41", "v42", "v43", "v44", "v45", "v46", "v47",
          "v48", "v49", "v50", "v51", "v52", "v53", "v54", "v55", "v56", "v57", "v58", "v59", "v60", "v61", "v62", "v63", "v64", "v65", "v66", "v67", "v68", "v69", "v70", "v71",
          "v72", "v73", "v74", "v75", "v76", "v77", "v78", "v79", "v80", "v81", "v82", "v83", "v84", "v85", "v86", "v87", "v88", "v89", "v90", "v91", "v92", "v93", "v94", "v95",
          "v96", "v97", "v98", "a0", "a1", "a2", "a3", "a4", "a5", "a6", "a7", "a8", "a9", "a10", "a11", "a12", "a13", "a14", "a15", "a16", "a17", "a18", "a19", "a20",
          "a21", "a22", "a23", "a24", "a25", "a26", "a27", "a28", "a29", "a30", "a31", "a32", "a33", "a34", "a35", "a36", "a37", "a38", "a39", "a40", "a41", "a42", "a43", "a44",
          "a45", "a46", "a47", "a48", "a49", "a50", "a51", "a52", "a53", "a54", "a55", "s40", "s41", "s42", "s43", "s44", "s45", "s46", "s47", "s48", "s49", "s50", "s51", "s52",
          "s53", "s54", "s55", "s56", "s57", "s58", "s59", "s60", "s61", "s62", "s63", "s64", "s65", "s66", "s67", "s68", "s69", "s70", "s71", "s72", "s73", "s74", "s75", "s76",
          "s77", "s78", "s79", "s80", "s81", "s82", "s83", "vcc", "scc", "memory");
    }
    bar();
    if (w == 0 && lane == 0) flag_set(wr, F_D + (h >> 2) * 128 + ((h & 3) * 4 + j) * 4);
    FQ(80 + h * 4 + j, 0);
    if (w == 0 || w == 3) DUMP(56 + (h * 4 + j) * 2 + (w == 3));
}

DEV void x_worker(unsigned char* ws, u32x4 rw, u32x4 rh, unsigned t0, int h, int w, int lane) {
    const int n = lane & 15, kg = lane >> 4, i8 = n & 7, up = n >> 3;
    const unsigned xc = h & 3, ob = memoff(i8, 4 * (4 * w + up) + kg);
    const int key0 = 16 * (n >> 3) + 4 * ((n >> 2) & 1) + (n & 3);
    const unsigned vk = (unsigned)O_KT + h * 28672 + (16 * w + kg) * 512 + key0 * 16;
    if (w < 3) {
        asm volatile(
        "s_mov_b32 s50, 0xff00ff\n\t"
        "s_mov_b32 s51, 0xff00ff\n\t"
        "s_mov_b32 s40, 0\n\t"
        "s_mov_b32 s43, %[bud]\n\t"
        "s_mov_b32 s55, 0\n\t"
        "s_mov_b64 s[46:47], 0\n\t"
        "s_mov_b32 s77, %[t0]\n\t"
        "v_mov_b32 v76, %[lw]\n\t"
        "v_mov_b32 v77, %[l16]\n\t"
        "v_mbcnt_lo_u32_b32 v0, -1, 0\n\t"
        "v_mbcnt_hi_u32_b32 v0, -1, v0\n\t"
        "v_lshlrev_b32 v0, 2, v0\n\t"
        "s_mov_b32 s82, 132\n\t"
        "s_mov_b32 s83, 62\n\t"
        "LPA_%=:\n\t"
        "buffer_load_dword v2, off, %[rw], %[fr] offset:1536 sc1\n\t"
        "s_waitcnt vmcnt(0)\n\t"
        "v_cmp_eq_u32 vcc, 0, v2\n\t"
        "s_cbranch_vccz LPB_%=\n\t"
        "s_sleep 8\n\t"
        "s_sub_u32 s43, s43, 1\n\t"
        "s_cbranch_scc0 LPA_%=\n\t"
        "s_mov_b32 s43, 0\n\t"
        "LPB_%=:\n\t"
        "buffer_load_dword v2, v0, %[rw], %[fr] offen sc1\n\t"
        "s_waitcnt vmcnt(0)\n\t"
        "v_cmp_eq_u32_e64 s[80:81], 2, v2\n\t"
        "LFp_%=:\n\t"
        "s_bitcmp1_b64 s[46:47], s40\n\t"
        "s_cbranch_scc1 LGp_%=\n\t"
        "buffer_load_dword v0, %[vl4], %[rw], %[fo] offen sc1\n\t"
        "s_waitcnt vmcnt(0)\n\t"
        "v_cmp_ne_u32_e64 s[46:47], 0, v0\n\t"
        "s_sub_u32 s43, s43, 1\n\t"
        "s_cbranch_scc1 LDp_%=\n\t"
        "s_sleep 1\n\t"
        "s_branch LFp_%=\n\t"
        "LDp_%=:\n\t"
        "s_mov_b32 s43, 0\n\t"
        "LGp_%=:\n\t"
        "s_add_u32 s44, s55, 0\n\t"
        "buffer_load_dwordx4 v[16:19], %[vk], %[rw], s44 offen sc1\n\t"
        "buffer_load_dwordx4 v[32:35], %[vk], %[rw], s44 offen offset:128 sc1\n\t"
        "s_add_u32 s44, s55, 2048\n\t"
        "buffer_load_dwordx4 v[20:23], %[vk], %[rw], s44 offen sc1\n\t"
        "buffer_load_dwordx4 v[36:39], %[vk], %[rw], s44 offen offset:128 sc1\n\t"
        "s_add_u32 s44, s55, 4096\n\t"
        "buffer_load_dwordx4 v[24:27], %[vk], %[rw], s44 offen sc1\n\t"
        "buffer_load_dwordx4 v[40:43], %[vk], %[rw], s44 offen offset:128 sc1\n\t"
        "s_add_u32 s44, s55, 6144\n\t"
        "buffer_load_dwordx4 v[28:31], %[vk], %[rw], s44 offen sc1\n\t"
        "buffer_load_dwordx4 v[44:47], %[vk], %[rw], s44 offen offset:128 sc1\n\t"
        "s_bitcmp1_b64 s[80:81], s40\n\t"
        "s_cselect_b32 s44, 8192, 0\n\t"
        "s_lshl_b32 s41, s40, 18\n\t"
        "s_add_u32 s41, s41, s44\n\t"
        "LOOP_%=:\n\t"
        "s_add_u32 s56, s41, %[c0]\n\t"
        "s_add_u32 s42, s77, 0\n\t"
        "LUx_%=:\n\t"
        "s_memrealtime s[78:79]\n\t"
        "s_waitcnt lgkmcnt(0)\n\t"
        "s_sub_u32 s78, s78, s42\n\t"
        "s_cmp_lt_i32 s78, 0\n\t"
        "s_cbranch_scc1 LUx_%=\n\t"
        "s_cmp_ge_u32 s78, 2\n\t"
        "s_cbranch_scc1 LVx_%=\n\t"
        "s_nop 7\n\t"
        "s_nop 6\n\t"
        "LVx_%=:\n\t"
        "s_bitcmp1_b32 s78, 0\n\t"
        "s_cbranch_scc1 LWx_%=\n\t"
        "s_nop 7\n\t"
        "s_nop 0\n\t"
        "LWx_%=:\n\t"
        "buffer_load_dwordx4 v[0:3], %[ob], %[rh], s56 offen sc1\n\t"
        "buffer_load_dwordx4 v[4:7], %[ob], %[rh], s56 offen offset:1024 sc1\n\t"
        "s_waitcnt vmcnt(1)\n\t"
        "v_mfma_f32_16x16x32_bf16 v[48:51], v[16:19], v[0:3], 0\n\t"
        "s_waitcnt vmcnt(0)\n\t"
        "v_max3_u32 v56, v0, v1, v2\n\t"
        "v_max3_u32 v56, v56, v3, v4\n\t"
        "v_max3_u32 v56, v56, v5, v6\n\t"
        "v_mfma_f32_16x16x32_bf16 v[52:55], v[32:35], v[0:3], 0\n\t"
        "v_max_u32 v56, v56, v7\n\t"
        "v_cmp_eq_u32 vcc, -1, v56\n\t"
        "s_cbranch_vccnz LRx_%=\n\t"
        "LGOx_%=:\n\t"
        "v_mfma_f32_16x16x32_bf16 v[48:51], v[24:27], v[4:7], v[48:51]\n\t"
        "v_mov_b32_dpp v8, v0 row_ror:8 row_mask:0xf bank_mask:0xf bound_ctrl:1\n\t"
        "v_mov_b32_dpp v9, v1 row_ror:8 row_mask:0xf bank_mask:0xf bound_ctrl:1\n\t"
        "v_mov_b32_dpp v10, v2 row_ror:8 row_mask:0xf bank_mask:0xf bound_ctrl:1\n\t"
        "v_mfma_f32_16x16x32_bf16 v[52:55], v[40:43], v[4:7], v[52:55]\n\t"
        "v_mov_b32_dpp v11, v3 row_ror:8 row_mask:0xf bank_mask:0xf bound_ctrl:1\n\t"
        "v_mov_b32_dpp v12, v4 row_ror:8 row_mask:0xf bank_mask:0xf bound_ctrl:1\n\t"
        "v_mov_b32_dpp v13, v5 row_ror:8 row_mask:0xf bank_mask:0xf bound_ctrl:1\n\t"
        "v_mfma_f32_16x16x32_bf16 v[48:51], v[20:23], v[8:11], v[48:51]\n\t"
        "v_mov_b32_dpp v14, v6 row_ror:8 row_mask:0xf bank_mask:0xf bound_ctrl:1\n\t"
        "v_mov_b32_dpp v15, v7 row_ror:8 row_mask:0xf bank_mask:0xf bound_ctrl:1\n\t"
        "v_mfma_f32_16x16x32_bf16 v[52:55], v[36:39], v[8:11], v[52:55]\n\t"
        "s_nop 0\n\t"
        "v_mfma_f32_16x16x32_bf16 v[48:51], v[28:31], v[12:15], v[48:51]\n\t"
        "v_mfma_f32_16x16x32_bf16 v[52:55], v[44:47], v[12:15], v[52:55]\n\t"
        "s_mov_b64 exec, s[50:51]\n\t"
        "s_nop 5\n\t"
        "ds_write_b128 v76, v[48:51]\n\t"
        "ds_write_b128 v76, v[52:55] offset:128\n\t"
        "s_mov_b64 exec, -1\n\t"
        "s_waitcnt lgkmcnt(0)\n\t"
        "s_barrier\n\t"
        "s_sleep 11\n\t"
        "s_add_u32 s48, s40, 1\n\t"
        "s_add_u32 s55, s55, 458752\n\t"
        "s_cmp_lt_u32 s48, 63\n\t"
        "s_cbranch_scc0 LEND_%=\n\t"
        "LFq_%=:\n\t"
        "s_bitcmp1_b64 s[46:47], s48\n\t"
        "s_cbranch_scc1 LGq_%=\n\t"
        "buffer_load_dword v74, %[vl4], %[rw], %[fo] offen sc1\n\t"
        "s_waitcnt vmcnt(0)\n\t"
        "v_cmp_ne_u32_e64 s[46:47], 0, v74\n\t"
        "s_sub_u32 s43, s43, 1\n\t"
        "s_cbranch_scc1 LDq_%=\n\t"
        "s_sleep 1\n\t"
        "s_branch LFq_%=\n\t"
        "LDq_%=:\n\t"
        "s_mov_b32 s43, 0\n\t"
        "LGq_%=:\n\t"
        "buffer_load_dword v75, %[ots], %[rh], s41 offen sc1\n\t"
        "buffer_load_dword v78, %[vl4], %[rw], %[fo] offen sc1\n\t"
        "s_add_u32 s44, s55, 0\n\t"
        "buffer_load_dwordx4 v[16:19], %[vk], %[rw], s44 offen sc1\n\t"
        "buffer_load_dwordx4 v[32:35], %[vk], %[rw], s44 offen offset:128 sc1\n\t"
        "s_add_u32 s44, s55, 2048\n\t"
        "buffer_load_dwordx4 v[20:23], %[vk], %[rw], s44 offen sc1\n\t"
        "buffer_load_dwordx4 v[36:39], %[vk], %[rw], s44 offen offset:128 sc1\n\t"
        "s_add_u32 s44, s55, 4096\n\t"
        "buffer_load_dwordx4 v[24:27], %[vk], %[rw], s44 offen sc1\n\t"
        "buffer_load_dwordx4 v[40:43], %[vk], %[rw], s44 offen offset:128 sc1\n\t"
        "s_add_u32 s44, s55, 6144\n\t"
        "buffer_load_dwordx4 v[28:31], %[vk], %[rw], s44 offen sc1\n\t"
        "buffer_load_dwordx4 v[44:47], %[vk], %[rw], s44 offen offset:128 sc1\n\t"
        "s_waitcnt vmcnt(8)\n\t"
        "v_cmp_ne_u32_e64 s[46:47], 0, v78\n\t"
        "LTA_%=:\n\t"
        "v_max_u32_dpp v75, v75, v75 row_shr:1 row_mask:0xf bank_mask:0xf bound_ctrl:1\n\t"
        "s_nop 1\n\t"
        "v_max_u32_dpp v75, v75, v75 row_shr:2 row_mask:0xf bank_mask:0xf bound_ctrl:1\n\t"
        "s_nop 1\n\t"
        "v_max_u32_dpp v75, v75, v75 row_shr:4 row_mask:0xf bank_mask:0xf bound_ctrl:1\n\t"
        "s_nop 1\n\t"
        "v_max_u32_dpp v75, v75, v75 row_shr:8 row_mask:0xf bank_mask:0xf bound_ctrl:1\n\t"
        "s_nop 0\n\t"
        "v_readlane_b32 s44, v75, 15\n\t"
        "v_readlane_b32 s42, v75, 31\n\t"
        "s_nop 1\n\t"
        "s_max_u32 s44, s44, s42\n\t"
        "s_cmp_eq_u32 s44, -1\n\t"
        "s_cbranch_scc1 LTS_%=\n\t"
        "s_add_u32 s77, s77, s44\n\t"
        "s_cmp_ge_u32 s40, 6\n\t"
        "s_cselect_b32 s42, s44, 0\n\t"
        "s_cmp_lg_u32 s42, 0\n\t"
        "s_cselect_b32 s42, 2, 0\n\t"
        "s_add_u32 s82, s82, s42\n\t"
        "s_min_u32 s82, s82, 136\n\t"
        "LTB_%=:\n\t"
        "s_add_u32 s77, s77, s82\n\t"
        "s_mov_b32 s40, s48\n\t"
        "v_xor_b32 v76, 4096, v76\n\t"
        "v_xor_b32 v77, 4096, v77\n\t"
        "s_bitcmp1_b64 s[80:81], s40\n\t"
        "s_cselect_b32 s44, 8192, 0\n\t"
        "s_lshl_b32 s41, s40, 18\n\t"
        "s_add_u32 s41, s41, s44\n\t"
        "s_branch LOOP_%=\n\t"
        "LEND_%=:\n\t"
        "s_waitcnt vmcnt(0)\n\t"
        "s_branch LOUT_%=\n\t"
        "LRx_%=:\n\t"
        "s_mov_b32 s59, %[c1]\n\t"
        "s_mov_b32 s58, %[c2]\n\t"
        "LPx_%=:\n\t"
        "s_mov_b32 s57, s58\n\t"
        "s_mov_b32 s58, %[c3]\n\t"
        "s_add_u32 s56, s41, s59\n\t"
        "s_mov_b32 s59, s57\n\t"
        "buffer_load_dwordx4 v[0:3], %[ob], %[rh], s56 offen sc1\n\t"
        "buffer_load_dwordx4 v[4:7], %[ob], %[rh], s56 offen offset:1024 sc1\n\t"
        "s_waitcnt vmcnt(0)\n\t"
        "v_max3_u32 v56, v0, v1, v2\n\t"
        "v_max3_u32 v56, v56, v3, v4\n\t"
        "v_max3_u32 v56, v56, v5, v6\n\t"
        "v_max_u32 v56, v56, v7\n\t"
        "v_cmp_eq_u32 vcc, -1, v56\n\t"
        "s_cbranch_vccz LKx_%=\n\t"
        "s_sub_u32 s43, s43, 1\n\t"
        "s_cbranch_scc0 LPx_%=\n\t"
        "s_mov_b32 s43, 0\n\t"
        "v_mov_b32 v0, 0\n\t"
        "v_mov_b32 v1, 0\n\t"
        "v_mov_b32 v2, 0\n\t"
        "v_mov_b32 v3, 0\n\t"
        "v_mov_b32 v4, 0\n\t"
        "v_mov_b32 v5, 0\n\t"
        "v_mov_b32 v6, 0\n\t"
        "v_mov_b32 v7, 0\n\t"
        "LKx_%=:\n\t"
        "s_nop 1\n\t"
        "v_mfma_f32_16x16x32_bf16 v[48:51], v[16:19], v[0:3], 0\n\t"
        "v_mfma_f32_16x16x32_bf16 v[52:55], v[32:35], v[0:3], 0\n\t"
        "s_branch LGOx_%=\n\t"
        "LTS_%=:\n\t"
        "buffer_load_dword v75, %[ots], %[rh], s41 offen sc1\n\t"
        "s_waitcnt vmcnt(0)\n\t"
        "s_sub_u32 s43, s43, 1\n\t"
        "s_cbranch_scc0 LTA_%=\n\t"
        "s_mov_b32 s43, 0\n\t"
        "s_branch LTB_%=\n\t"
        "LOUT_%=:\n\t"
        :: VDBG [rw] "s"(rw), [rh] "s"(rh), [fr] "s"((unsigned)O_FLG + F_R), [bud] "s"(SPIN_LIMIT), RUNGS, OTS, [fo] "s"((unsigned)O_FLG + F_X + xc * 256), [vl4] "v"(lane * 4), [vk] "v"(vk), [ob] "v"(ob),
           [lw] "v"(w * 1024 + kg * 256 + n * 16), [l16] "v"(lane * 16)
        :
          "v0", "v1", "v2", "v3", "v4", "v5", "v6", "v7", "v8", "v9", "v10", "v11", "v12", "v13", "v14", "v15", "v16", "v17", "v18", "v19", "v20", "v21", "v22", "v23",
          "v24", "v25", "v26", "v27", "v28", "v29", "v30", "v31", "v32", "v33", "v34", "v35", "v36", "v37", "v38", "v39", "v40", "v41", "v42", "v43", "v44", "v45", "v46", "v47",
          "v48", "v49", "v50", "v51", "v52", "v53", "v54", "v55", "v56", "v57", "v58", "v59", "v60", "v61", "v62", "v63", "v64", "v65", "v66", "v67", "v68", "v69", "v70", "v71",
          "v72", "v73", "v74", "v75", "v76", "v77", "v78", "s40", "s41", "s42", "s43", "s44", "s45", "s46", "s47", "s48", "s49", "s50", "s51", "s52", "s53", "s54", "s55", "s56",
          "s57", "s58", "s59", "s60", "s61", "s62", "s63", "s64", "s65", "s66", "s67", "s68", "s69", "s70", "s71", "s72", "s73", "s74", "s75", "s76", "s77", "s78", "s79", "s80",
          "s81", "s82", "s83", "vcc", "scc", "memory");
    } else {
        asm volatile(
        "s_mov_b32 s50, 0xff00ff\n\t"
        "s_mov_b32 s51, 0xff00ff\n\t"
        "s_mov_b32 s40, 0\n\t"
        "s_mov_b32 s43, %[bud]\n\t"
        "s_mov_b32 s55, 0\n\t"
        "s_mov_b64 s[46:47], 0\n\t"
        "s_mov_b32 s77, %[t0]\n\t"
        "v_mov_b32 v76, %[lw]\n\t"
        "v_mov_b32 v77, %[l16]\n\t"
        "v_mbcnt_lo_u32_b32 v0, -1, 0\n\t"
        "v_mbcnt_hi_u32_b32 v0, -1, v0\n\t"
        "v_lshlrev_b32 v0, 2, v0\n\t"
        "s_mov_b32 s82, 132\n\t"
        "s_mov_b32 s83, 62\n\t"
        "LPA_%=:\n\t"
        "buffer_load_dword v2, off, %[rw], %[fr] offset:1536 sc1\n\t"
        "s_waitcnt vmcnt(0)\n\t"
        "v_cmp_eq_u32 vcc, 0, v2\n\t"
        "s_cbranch_vccz LPB_%=\n\t"
        "s_sleep 8\n\t"
        "s_sub_u32 s43, s43, 1\n\t"
        "s_cbranch_scc0 LPA_%=\n\t"
        "s_mov_b32 s43, 0\n\t"
        "LPB_%=:\n\t"
        "buffer_load_dword v2, v0, %[rw], %[fr] offen sc1\n\t"
        "s_waitcnt vmcnt(0)\n\t"
        "v_cmp_eq_u32_e64 s[80:81], 2, v2\n\t"
        "LFp_%=:\n\t"
        "s_bitcmp1_b64 s[46:47], s40\n\t"
        "s_cbranch_scc1 LGp_%=\n\t"
        "buffer_load_dword v0, %[vl4], %[rw], %[fo] offen sc1\n\t"
        "s_waitcnt vmcnt(0)\n\t"
        "v_cmp_ne_u32_e64 s[46:47], 0, v0\n\t"
        "s_sub_u32 s43, s43, 1\n\t"
        "s_cbranch_scc1 LDp_%=\n\t"
        "s_sleep 1\n\t"
        "s_branch LFp_%=\n\t"
        "LDp_%=:\n\t"
        "s_mov_b32 s43, 0\n\t"
        "LGp_%=:\n\t"
        "s_add_u32 s44, s55, 0\n\t"
        "buffer_load_dwordx4 v[16:19], %[vk], %[rw], s44 offen sc1\n\t"
        "buffer_load_dwordx4 v[24:27], %[vk], %[rw], s44 offen offset:128 sc1\n\t"
        "s_add_u32 s44, s55, 2048\n\t"
        "buffer_load_dwordx4 v[20:23], %[vk], %[rw], s44 offen sc1\n\t"
        "buffer_load_dwordx4 v[28:31], %[vk], %[rw], s44 offen offset:128 sc1\n\t"
        "s_bitcmp1_b64 s[80:81], s40\n\t"
        "s_cselect_b32 s44, 8192, 0\n\t"
        "s_lshl_b32 s41, s40, 18\n\t"
        "s_add_u32 s41, s41, s44\n\t"
        "LOOP_%=:\n\t"
        "s_add_u32 s45, s41, 131072\n\t"
        "s_add_u32 s56, s41, %[c0]\n\t"
        "s_add_u32 s42, s77, 0\n\t"
        "LUx_%=:\n\t"
        "s_memrealtime s[78:79]\n\t"
        "s_waitcnt lgkmcnt(0)\n\t"
        "s_sub_u32 s78, s78, s42\n\t"
        "s_cmp_lt_i32 s78, 0\n\t"
        "s_cbranch_scc1 LUx_%=\n\t"
        "s_cmp_ge_u32 s78, 2\n\t"
        "s_cbranch_scc1 LVx_%=\n\t"
        "s_nop 7\n\t"
        "s_nop 6\n\t"
        "LVx_%=:\n\t"
        "s_bitcmp1_b32 s78, 0\n\t"
        "s_cbranch_scc1 LWx_%=\n\t"
        "s_nop 7\n\t"
        "s_nop 0\n\t"
        "LWx_%=:\n\t"
        "buffer_load_dwordx4 v[0:3], %[ob], %[rh], s56 offen sc1\n\t"
        "s_waitcnt vmcnt(0)\n\t"
        "v_mfma_f32_16x16x32_bf16 v[48:51], v[16:19], v[0:3], 0\n\t"
        "v_max3_u32 v56, v0, v1, v2\n\t"
        "v_max_u32 v56, v56, v3\n\t"
        "v_cmp_eq_u32 vcc, -1, v56\n\t"
        "v_mfma_f32_16x16x32_bf16 v[52:55], v[24:27], v[0:3], 0\n\t"
        "s_cbranch_vccnz LRx_%=\n\t"
        "LGOx_%=:\n\t"
        "v_mov_b32_dpp v8, v0 row_ror:8 row_mask:0xf bank_mask:0xf bound_ctrl:1\n\t"
        "v_mov_b32_dpp v9, v1 row_ror:8 row_mask:0xf bank_mask:0xf bound_ctrl:1\n\t"
        "v_mov_b32_dpp v10, v2 row_ror:8 row_mask:0xf bank_mask:0xf bound_ctrl:1\n\t"
        "v_mov_b32_dpp v11, v3 row_ror:8 row_mask:0xf bank_mask:0xf bound_ctrl:1\n\t"
        "s_nop 1\n\t"
        "v_mfma_f32_16x16x32_bf16 v[48:51], v[20:23], v[8:11], v[48:51]\n\t"
        "v_mfma_f32_16x16x32_bf16 v[52:55], v[28:31], v[8:11], v[52:55]\n\t"
        "s_mov_b64 exec, s[50:51]\n\t"
        "s_nop 5\n\t"
        "ds_write_b128 v76, v[48:51]\n\t"
        "ds_write_b128 v76, v[52:55] offset:128\n\t"
        "s_mov_b64 exec, -1\n\t"
        "s_waitcnt lgkmcnt(0)\n\t"
        "s_barrier\n\t"
        "ds_read_b128 v[56:59], v77 offset:0\n\t"
        "ds_read_b128 v[60:63], v77 offset:1024\n\t"
        "ds_read_b128 v[64:67], v77 offset:2048\n\t"
        "ds_read_b128 v[68:71], v77 offset:3072\n\t"
        "s_waitcnt lgkmcnt(2)\n\t"
        "v_pk_add_f32 v[56:57], v[56:57], v[60:61]\n\t"
        "v_pk_add_f32 v[58:59], v[58:59], v[62:63]\n\t"
        "s_waitcnt lgkmcnt(1)\n\t"
        "v_pk_add_f32 v[56:57], v[56:57], v[64:65]\n\t"
        "v_pk_add_f32 v[58:59], v[58:59], v[66:67]\n\t"
        "s_waitcnt lgkmcnt(0)\n\t"
        "v_pk_add_f32 v[56:57], v[56:57], v[68:69]\n\t"
        "v_pk_add_f32 v[58:59], v[58:59], v[70:71]\n\t"
        "v_max3_f32 v66, v56, v57, v58\n\t"
        "v_max_f32 v66, v66, v59\n\t"
        "s_nop 1\n\t"
        "v_max_f32_dpp v62, v66, v66 row_ror:8 row_mask:0xf bank_mask:0xf bound_ctrl:1\n\t"
        "v_max_f32_dpp v66, v66, v66 row_ror:8 row_mask:0xf bank_mask:0xf bound_ctrl:1\n\t"
        "s_nop 1\n\t"
        "v_permlane16_swap_b32 v66, v62\n\t"
        "v_max_f32 v61, v66, v62\n\t"
        "v_max_f32 v66, v66, v62\n\t"
        "s_nop 1\n\t"
        "v_permlane32_swap_b32 v66, v61\n\t"
        "v_max_f32 v66, v66, v61\n\t"
        "v_sub_f32 v56, v56, v66\n\t"
        "v_exp_f32 v56, v56\n\t"
        "v_sub_f32 v57, v57, v66\n\t"
        "v_exp_f32 v57, v57\n\t"
        "v_sub_f32 v58, v58, v66\n\t"
        "v_exp_f32 v58, v58\n\t"
        "v_sub_f32 v59, v59, v66\n\t"
        "v_exp_f32 v59, v59\n\t"
        "v_cvt_pk_bf16_f32 v64, v56, v57\n\t"
        "v_add_f32 v67, v56, v57\n\t"
        "v_cvt_pk_bf16_f32 v65, v58, v59\n\t"
        "v_min_u32 v70, -2, v64\n\t"
        "v_min_u32 v71, -2, v65\n\t"
        "v_min_u32 v68, -2, v64\n\t"
        "v_min_u32 v69, -2, v65\n\t"
        "v_mov_b32_dpp v68, v70 row_ror:8 row_mask:0xf bank_mask:0xc\n\t"
        "v_mov_b32_dpp v69, v71 row_ror:8 row_mask:0xf bank_mask:0xc\n\t"
        "v_mov_b32_dpp v70, v70 row_ror:8 row_mask:0xf bank_mask:0x3\n\t"
        "v_mov_b32_dpp v71, v71 row_ror:8 row_mask:0xf bank_mask:0x3\n\t"
        "buffer_store_dwordx4 v[68:71], %[oe], %[rh], s41 offen sc1\n\t"
        "buffer_store_dwordx4 v[68:71], %[oe], %[rh], s45 offen sc1\n\t"
        "v_add_f32 v67, v67, v58\n\t"
        "v_add_f32 v67, v67, v59\n\t"
        "v_min_u32 v66, -2, v66\n\t"
        "s_nop 0\n\t"
        "v_add_f32_dpp v63, v67, v67 row_ror:8 row_mask:0xf bank_mask:0xf bound_ctrl:1\n\t"
        "v_add_f32_dpp v67, v67, v67 row_ror:8 row_mask:0xf bank_mask:0xf bound_ctrl:1\n\t"
        "s_nop 1\n\t"
        "v_permlane16_swap_b32 v67, v63\n\t"
        "v_add_f32 v61, v67, v63\n\t"
        "v_add_f32 v67, v67, v63\n\t"
        "s_nop 1\n\t"
        "v_permlane32_swap_b32 v67, v61\n\t"
        "v_add_f32 v67, v67, v61\n\t"
        "s_mov_b32 exec_hi, 0\n\t"
        "v_min_u32 v67, -2, v67\n\t"
        "buffer_store_dwordx2 v[66:67], %[ost], %[rh], s41 offen sc1\n\t"
        "s_mov_b32 exec_hi, -1\n\t"
        "s_add_u32 s48, s40, 1\n\t"
        "s_add_u32 s55, s55, 458752\n\t"
        "s_cmp_lt_u32 s48, 63\n\t"
        "s_cbranch_scc0 LEND_%=\n\t"
        "LFq_%=:\n\t"
        "s_bitcmp1_b64 s[46:47], s48\n\t"
        "s_cbranch_scc1 LGq_%=\n\t"
        "buffer_load_dword v74, %[vl4], %[rw], %[fo] offen sc1\n\t"
        "s_waitcnt vmcnt(0)\n\t"
        "v_cmp_ne_u32_e64 s[46:47], 0, v74\n\t"
        "s_sub_u32 s43, s43, 1\n\t"
        "s_cbranch_scc1 LDq_%=\n\t"
        "s_sleep 1\n\t"
        "s_branch LFq_%=\n\t"
        "LDq_%=:\n\t"
        "s_mov_b32 s43, 0\n\t"
        "LGq_%=:\n\t"
        "buffer_load_dword v75, %[ots], %[rh], s41 offen sc1\n\t"
        "buffer_load_dword v78, %[vl4], %[rw], %[fo] offen sc1\n\t"
        "s_add_u32 s44, s55, 0\n\t"
        "buffer_load_dwordx4 v[16:19], %[vk], %[rw], s44 offen sc1\n\t"
        "buffer_load_dwordx4 v[24:27], %[vk], %[rw], s44 offen offset:128 sc1\n\t"
        "s_add_u32 s44, s55, 2048\n\t"
        "buffer_load_dwordx4 v[20:23], %[vk], %[rw], s44 offen sc1\n\t"
        "buffer_load_dwordx4 v[28:31], %[vk], %[rw], s44 offen offset:128 sc1\n\t"
        "s_waitcnt vmcnt(4)\n\t"
        "v_cmp_ne_u32_e64 s[46:47], 0, v78\n\t"
        "LTA_%=:\n\t"
        "v_max_u32_dpp v75, v75, v75 row_shr:1 row_mask:0xf bank_mask:0xf bound_ctrl:1\n\t"
        "s_nop 1\n\t"
        "v_max_u32_dpp v75, v75, v75 row_shr:2 row_mask:0xf bank_mask:0xf bound_ctrl:1\n\t"
        "s_nop 1\n\t"
        "v_max_u32_dpp v75, v75, v75 row_shr:4 row_mask:0xf bank_mask:0xf bound_ctrl:1\n\t"
        "s_nop 1\n\t"
        "v_max_u32_dpp v75, v75, v75 row_shr:8 row_mask:0xf bank_mask:0xf bound_ctrl:1\n\t"
        "s_nop 0\n\t"
        "v_readlane_b32 s44, v75, 15\n\t"
        "v_readlane_b32 s42, v75, 31\n\t"
        "s_nop 1\n\t"
        "s_max_u32 s44, s44, s42\n\t"
        "s_cmp_eq_u32 s44, -1\n\t"
        "s_cbranch_scc1 LTS_%=\n\t"
        "s_add_u32 s77, s77, s44\n\t"
        "s_cmp_ge_u32 s40, 6\n\t"
        "s_cselect_b32 s42, s44, 0\n\t"
        "s_cmp_lg_u32 s42, 0\n\t"
        "s_cselect_b32 s42, 2, 0\n\t"
        "s_add_u32 s82, s82, s42\n\t"
        "s_min_u32 s82, s82, 136\n\t"
        "LTB_%=:\n\t"
        "s_add_u32 s77, s77, s82\n\t"
        "s_mov_b32 s40, s48\n\t"
        "v_xor_b32 v76, 4096, v76\n\t"
        "v_xor_b32 v77, 4096, v77\n\t"
        "s_bitcmp1_b64 s[80:81], s40\n\t"
        "s_cselect_b32 s44, 8192, 0\n\t"
        "s_lshl_b32 s41, s40, 18\n\t"
        "s_add_u32 s41, s41, s44\n\t"
        "s_branch LOOP_%=\n\t"
        "LEND_%=:\n\t"
        "s_waitcnt vmcnt(0)\n\t"
        "s_branch LOUT_%=\n\t"
        "LRx_%=:\n\t"
        "s_mov_b32 s59, %[c1]\n\t"
        "s_mov_b32 s58, %[c2]\n\t"
        "LPx_%=:\n\t"
        "s_mov_b32 s57, s58\n\t"
        "s_mov_b32 s58, %[c3]\n\t"
        "s_add_u32 s56, s41, s59\n\t"
        "s_mov_b32 s59, s57\n\t"
        "buffer_load_dwordx4 v[0:3], %[ob], %[rh], s56 offen sc1\n\t"
        "s_waitcnt vmcnt(0)\n\t"
        "v_max3_u32 v56, v0, v1, v2\n\t"
        "v_max_u32 v56, v56, v3\n\t"
        "v_cmp_eq_u32 vcc, -1, v56\n\t"
        "s_cbranch_vccz LKx_%=\n\t"
        "s_sub_u32 s43, s43, 1\n\t"
        "s_cbranch_scc0 LPx_%=\n\t"
        "s_mov_b32 s43, 0\n\t"
        "v_mov_b32 v0, 0\n\t"
        "v_mov_b32 v1, 0\n\t"
        "v_mov_b32 v2, 0\n\t"
        "v_mov_b32 v3, 0\n\t"
        "LKx_%=:\n\t"
        "s_nop 1\n\t"
        "v_mfma_f32_16x16x32_bf16 v[48:51], v[16:19], v[0:3], 0\n\t"
        "v_mfma_f32_16x16x32_bf16 v[52:55], v[24:27], v[0:3], 0\n\t"
        "s_branch LGOx_%=\n\t"
        "LTS_%=:\n\t"
        "buffer_load_dword v75, %[ots], %[rh], s41 offen sc1\n\t"
        "s_waitcnt vmcnt(0)\n\t"
        "s_sub_u32 s43, s43, 1\n\t"
        "s_cbranch_scc0 LTA_%=\n\t"
        "s_mov_b32 s43, 0\n\t"
        "s_branch LTB_%=\n\t"
        "LOUT_%=:\n\t"
        :: VDBG [rw] "s"(rw), [rh] "s"(rh), [fr] "s"((unsigned)O_FLG + F_R), [bud] "s"(SPIN_LIMIT), RUNGS, OTS, [fo] "s"((unsigned)O_FLG + F_X + xc * 256), [vl4] "v"(lane * 4), [vk] "v"(vk), [ob] "v"(ob),
           [lw] "v"(w * 1024 + kg * 256 + n * 16), [l16] "v"(lane * 16), [oe] "v"(up * HO_GRP + HO_EX + h * 512 + kg * 128 + i8 * 16), [ost] "v"((lane >> 3) * HO_GRP + HO_ST + h * 64 + i8 * 8)
        :
          "v0", "v1", "v2", "v3", "v4", "v5", "v6", "v7", "v8", "v9", "v10", "v11", "v12", "v13", "v14", "v15", "v16", "v17", "v18", "v19", "v20", "v21", "v22", "v23",
          "v24", "v25", "v26", "v27", "v28", "v29", "v30", "v31", "v32", "v33", "v34", "v35", "v36", "v37", "v38", "v39", "v40", "v41", "v42", "v43", "v44", "v45", "v46", "v47",
          "v48", "v49", "v50", "v51", "v52", "v53", "v54", "v55", "v56", "v57", "v58", "v59", "v60", "v61", "v62", "v63", "v64", "v65", "v66", "v67", "v68", "v69", "v70", "v71",
          "v72", "v73", "v74", "v75", "v76", "v77", "v78", "s40", "s41", "s42", "s43", "s44", "s45", "s46", "s47", "s48", "s49", "s50", "s51", "s52", "s53", "s54", "s55", "s56",
          "s57", "s58", "s59", "s60", "s61", "s62", "s63", "s64", "s65", "s66", "s67", "s68", "s69", "s70", "s71", "s72", "s73", "s74", "s75", "s76", "s77", "s78", "s79", "s80",
          "s81", "s82", "s83", "vcc", "scc", "memory");
        DUMP(184 + h);
    }
}

DEV void c_worker(unsigned char* ws, rsrc_t wr, u32x4 rw, u32x4 rh, unsigned t0, int c, int w, int lane) {
    const int n = lane & 15, kg = lane >> 4, i8 = n & 7, up = n >> 3;
    const unsigned vw = wtoff(7168 + 4 * w * 448 + 16 * c + n, kg), xc = c & 3;
    const unsigned om0 = (w * 112 + lane) * 16, tq = 7 * w + min(lane - 48, 6), om1 = lane < 48 ? om0 + 1024 : 7168 + tq * 16;
    const int hd = 4 * w + 2 * up + (kg >> 1), dsel = i8 - 4 * (kg & 1);
    const unsigned mz = dsel == 0 ? 0xFFFFu : dsel == 1 ? 0xFFFF0000u : 0u, mw = dsel == 2 ? 0xFFFFu : dsel == 3 ? 0xFFFF0000u : 0u;
    asm volatile(
        "s_mov_b32 s44, 0\n\t"
        "buffer_load_dwordx4 v[0:3], %[vw], %[rw], s44 offen offset:0\n\t"
        "buffer_load_dwordx4 v[4:7], %[vw], %[rw], s44 offen offset:1024\n\t"
        "buffer_load_dwordx4 v[8:11], %[vw], %[rw], s44 offen offset:2048\n\t"
        "buffer_load_dwordx4 v[12:15], %[vw], %[rw], s44 offen offset:3072\n\t"
        "s_mov_b32 s44, 4096\n\t"
        "buffer_load_dwordx4 v[16:19], %[vw], %[rw], s44 offen offset:0\n\t"
        "buffer_load_dwordx4 v[20:23], %[vw], %[rw], s44 offen offset:1024\n\t"
        "buffer_load_dwordx4 v[24:27], %[vw], %[rw], s44 offen offset:2048\n\t"
        "buffer_load_dwordx4 v[28:31], %[vw], %[rw], s44 offen offset:3072\n\t"
        "s_mov_b32 s44, 8192\n\t"
        "buffer_load_dwordx4 v[32:35], %[vw], %[rw], s44 offen offset:0\n\t"
        "buffer_load_dwordx4 v[36:39], %[vw], %[rw], s44 offen offset:1024\n\t"
        "buffer_load_dwordx4 v[40:43], %[vw], %[rw], s44 offen offset:2048\n\t"
        "buffer_load_dwordx4 v[44:47], %[vw], %[rw], s44 offen offset:3072\n\t"
        "s_mov_b32 s44, 12288\n\t"
        "buffer_load_dwordx4 v[48:51], %[vw], %[rw], s44 offen offset:0\n\t"
        "buffer_load_dwordx4 v[52:55], %[vw], %[rw], s44 offen offset:1024\n\t"
        "s_mov_b32 s44, 401408\n\t"
        "buffer_load_dwordx4 v[56:59], %[vw], %[rw], s44 offen offset:0\n\t"
        "buffer_load_dwordx4 v[60:63], %[vw], %[rw], s44 offen offset:1024\n\t"
        "buffer_load_dwordx4 v[64:67], %[vw], %[rw], s44 offen offset:2048\n\t"
        "buffer_load_dwordx4 v[68:71], %[vw], %[rw], s44 offen offset:3072\n\t"
        "s_mov_b32 s44, 405504\n\t"
        "buffer_load_dwordx4 v[72:75], %[vw], %[rw], s44 offen offset:0\n\t"
        "buffer_load_dwordx4 v[76:79], %[vw], %[rw], s44 offen offset:1024\n\t"
        "buffer_load_dwordx4 v[80:83], %[vw], %[rw], s44 offen offset:2048\n\t"
        "buffer_load_dwordx4 v[84:87], %[vw], %[rw], s44 offen offset:3072\n\t"
        "s_mov_b32 s44, 409600\n\t"
        "buffer_load_dwordx4 v[88:91], %[vw], %[rw], s44 offen offset:0\n\t"
        "buffer_load_dwordx4 v[92:95], %[vw], %[rw], s44 offen offset:1024\n\t"
        "buffer_load_dwordx4 v[96:99], %[vw], %[rw], s44 offen offset:2048\n\t"
        "buffer_load_dwordx4 v[100:103], %[vw], %[rw], s44 offen offset:3072\n\t"
        "s_mov_b32 s44, 413696\n\t"
        "buffer_load_dwordx4 v[104:107], %[vw], %[rw], s44 offen offset:0\n\t"
        "buffer_load_dwordx4 v[108:111], %[vw], %[rw], s44 offen offset:1024\n\t"
        "s_waitcnt vmcnt(14)\n\t"
        "v_permlane16_swap_b32 v0, v2\n\t"
        "v_permlane16_swap_b32 v1, v3\n\t"
        "v_permlane16_swap_b32 v4, v6\n\t"
        "v_permlane16_swap_b32 v5, v7\n\t"
        "v_permlane16_swap_b32 v8, v10\n\t"
        "v_permlane16_swap_b32 v9, v11\n\t"
        "v_permlane16_swap_b32 v12, v14\n\t"
        "v_permlane16_swap_b32 v13, v15\n\t"
        "v_permlane16_swap_b32 v16, v18\n\t"
        "v_permlane16_swap_b32 v17, v19\n\t"
        "v_permlane16_swap_b32 v20, v22\n\t"
        "v_permlane16_swap_b32 v21, v23\n\t"
        "v_permlane16_swap_b32 v24, v26\n\t"
        "v_permlane16_swap_b32 v25, v27\n\t"
        "v_permlane16_swap_b32 v28, v30\n\t"
        "v_permlane16_swap_b32 v29, v31\n\t"
        "v_permlane16_swap_b32 v32, v34\n\t"
        "v_permlane16_swap_b32 v33, v35\n\t"
        "v_permlane16_swap_b32 v36, v38\n\t"
        "v_permlane16_swap_b32 v37, v39\n\t"
        "v_permlane16_swap_b32 v40, v42\n\t"
        "v_permlane16_swap_b32 v41, v43\n\t"
        "v_permlane16_swap_b32 v44, v46\n\t"
        "v_permlane16_swap_b32 v45, v47\n\t"
        "v_permlane16_swap_b32 v48, v50\n\t"
        "v_permlane16_swap_b32 v49, v51\n\t"
        "v_permlane16_swap_b32 v52, v54\n\t"
        "v_permlane16_swap_b32 v53, v55\n\t"
        "v_accvgpr_write_b32 a0, v0\n\t"
        "v_accvgpr_write_b32 a1, v2\n\t"
        "v_accvgpr_write_b32 a2, v1\n\t"
        "v_accvgpr_write_b32 a3, v3\n\t"
        "v_accvgpr_write_b32 a4, v4\n\t"
        "v_accvgpr_write_b32 a5, v6\n\t"
        "v_accvgpr_write_b32 a6, v5\n\t"
        "v_accvgpr_write_b32 a7, v7\n\t"
        "v_accvgpr_write_b32 a8, v8\n\t"
        "v_accvgpr_write_b32 a9, v10\n\t"
        "v_accvgpr_write_b32 a10, v9\n\t"
        "v_accvgpr_write_b32 a11, v11\n\t"
        "v_accvgpr_write_b32 a12, v12\n\t"
        "v_accvgpr_write_b32 a13, v14\n\t"
        "v_accvgpr_write_b32 a14, v13\n\t"
        "v_accvgpr_write_b32 a15, v15\n\t"
        "v_accvgpr_write_b32 a16, v16\n\t"
        "v_accvgpr_write_b32 a17, v18\n\t"
        "v_accvgpr_write_b32 a18, v17\n\t"
        "v_accvgpr_write_b32 a19, v19\n\t"
        "v_accvgpr_write_b32 a20, v20\n\t"
        "v_accvgpr_write_b32 a21, v22\n\t"
        "v_accvgpr_write_b32 a22, v21\n\t"
        "v_accvgpr_write_b32 a23, v23\n\t"
        "v_accvgpr_write_b32 a24, v24\n\t"
        "v_accvgpr_write_b32 a25, v26\n\t"
        "v_accvgpr_write_b32 a26, v25\n\t"
        "v_accvgpr_write_b32 a27, v27\n\t"
        "v_accvgpr_write_b32 a28, v28\n\t"
        "v_accvgpr_write_b32 a29, v30\n\t"
        "v_accvgpr_write_b32 a30, v29\n\t"
        "v_accvgpr_write_b32 a31, v31\n\t"
        "v_accvgpr_write_b32 a32, v32\n\t"
        "v_accvgpr_write_b32 a33, v34\n\t"
        "v_accvgpr_write_b32 a34, v33\n\t"
        "v_accvgpr_write_b32 a35, v35\n\t"
        "v_accvgpr_write_b32 a36, v36\n\t"
        "v_accvgpr_write_b32 a37, v38\n\t"
        "v_accvgpr_write_b32 a38, v37\n\t"
        "v_accvgpr_write_b32 a39, v39\n\t"
        "v_accvgpr_write_b32 a40, v40\n\t"
        "v_accvgpr_write_b32 a41, v42\n\t"
        "v_accvgpr_write_b32 a42, v41\n\t"
        "v_accvgpr_write_b32 a43, v43\n\t"
        "v_accvgpr_write_b32 a44, v44\n\t"
        "v_accvgpr_write_b32 a45, v46\n\t"
        "v_accvgpr_write_b32 a46, v45\n\t"
        "v_accvgpr_write_b32 a47, v47\n\t"
        "v_accvgpr_write_b32 a48, v48\n\t"
        "v_accvgpr_write_b32 a49, v50\n\t"
        "v_accvgpr_write_b32 a50, v49\n\t"
        "v_accvgpr_write_b32 a51, v51\n\t"
        "v_accvgpr_write_b32 a52, v52\n\t"
        "v_accvgpr_write_b32 a53, v54\n\t"
        "v_accvgpr_write_b32 a54, v53\n\t"
        "v_accvgpr_write_b32 a55, v55\n\t"
        "s_mov_b32 s44, 802816\n\t"
        "buffer_load_dwordx4 v[0:3], %[vw], %[rw], s44 offen offset:0\n\t"
        "buffer_load_dwordx4 v[4:7], %[vw], %[rw], s44 offen offset:1024\n\t"
        "buffer_load_dwordx4 v[8:11], %[vw], %[rw], s44 offen offset:2048\n\t"
        "buffer_load_dwordx4 v[12:15], %[vw], %[rw], s44 offen offset:3072\n\t"
        "s_mov_b32 s44, 806912\n\t"
        "buffer_load_dwordx4 v[16:19], %[vw], %[rw], s44 offen offset:0\n\t"
        "buffer_load_dwordx4 v[20:23], %[vw], %[rw], s44 offen offset:1024\n\t"
        "buffer_load_dwordx4 v[24:27], %[vw], %[rw], s44 offen offset:2048\n\t"
        "buffer_load_dwordx4 v[28:31], %[vw], %[rw], s44 offen offset:3072\n\t"
        "s_mov_b32 s44, 811008\n\t"
        "buffer_load_dwordx4 v[32:35], %[vw], %[rw], s44 offen offset:0\n\t"
        "buffer_load_dwordx4 v[36:39], %[vw], %[rw], s44 offen offset:1024\n\t"
        "buffer_load_dwordx4 v[40:43], %[vw], %[rw], s44 offen offset:2048\n\t"
        "buffer_load_dwordx4 v[44:47], %[vw], %[rw], s44 offen offset:3072\n\t"
        "s_mov_b32 s44, 815104\n\t"
        "buffer_load_dwordx4 v[48:51], %[vw], %[rw], s44 offen offset:0\n\t"
        "buffer_load_dwordx4 v[52:55], %[vw], %[rw], s44 offen offset:1024\n\t"
        "s_waitcnt vmcnt(14)\n\t"
        "v_permlane16_swap_b32 v56, v58\n\t"
        "v_permlane16_swap_b32 v57, v59\n\t"
        "v_permlane16_swap_b32 v60, v62\n\t"
        "v_permlane16_swap_b32 v61, v63\n\t"
        "v_permlane16_swap_b32 v64, v66\n\t"
        "v_permlane16_swap_b32 v65, v67\n\t"
        "v_permlane16_swap_b32 v68, v70\n\t"
        "v_permlane16_swap_b32 v69, v71\n\t"
        "v_permlane16_swap_b32 v72, v74\n\t"
        "v_permlane16_swap_b32 v73, v75\n\t"
        "v_permlane16_swap_b32 v76, v78\n\t"
        "v_permlane16_swap_b32 v77, v79\n\t"
        "v_permlane16_swap_b32 v80, v82\n\t"
        "v_permlane16_swap_b32 v81, v83\n\t"
        "v_permlane16_swap_b32 v84, v86\n\t"
        "v_permlane16_swap_b32 v85, v87\n\t"
        "v_permlane16_swap_b32 v88, v90\n\t"
        "v_permlane16_swap_b32 v89, v91\n\t"
        "v_permlane16_swap_b32 v92, v94\n\t"
        "v_permlane16_swap_b32 v93, v95\n\t"
        "v_permlane16_swap_b32 v96, v98\n\t"
        "v_permlane16_swap_b32 v97, v99\n\t"
        "v_permlane16_swap_b32 v100, v102\n\t"
        "v_permlane16_swap_b32 v101, v103\n\t"
        "v_permlane16_swap_b32 v104, v106\n\t"
        "v_permlane16_swap_b32 v105, v107\n\t"
        "v_permlane16_swap_b32 v108, v110\n\t"
        "v_permlane16_swap_b32 v109, v111\n\t"
        "v_accvgpr_write_b32 a56, v56\n\t"
        "v_accvgpr_write_b32 a57, v58\n\t"
        "v_accvgpr_write_b32 a58, v57\n\t"
        "v_accvgpr_write_b32 a59, v59\n\t"
        "v_accvgpr_write_b32 a60, v60\n\t"
        "v_accvgpr_write_b32 a61, v62\n\t"
        "v_accvgpr_write_b32 a62, v61\n\t"
        "v_accvgpr_write_b32 a63, v63\n\t"
        "v_accvgpr_write_b32 a64, v64\n\t"
        "v_accvgpr_write_b32 a65, v66\n\t"
        "v_accvgpr_write_b32 a66, v65\n\t"
        "v_accvgpr_write_b32 a67, v67\n\t"
        "v_accvgpr_write_b32 a68, v68\n\t"
        "v_accvgpr_write_b32 a69, v70\n\t"
        "v_accvgpr_write_b32 a70, v69\n\t"
        "v_accvgpr_write_b32 a71, v71\n\t"
        "v_accvgpr_write_b32 a72, v72\n\t"
        "v_accvgpr_write_b32 a73, v74\n\t"
        "v_accvgpr_write_b32 a74, v73\n\t"
        "v_accvgpr_write_b32 a75, v75\n\t"
        "v_accvgpr_write_b32 a76, v76\n\t"
        "v_accvgpr_write_b32 a77, v78\n\t"
        "v_accvgpr_write_b32 a78, v77\n\t"
        "v_accvgpr_write_b32 a79, v79\n\t"
        "v_accvgpr_write_b32 a80, v80\n\t"
        "v_accvgpr_write_b32 a81, v82\n\t"
        "v_accvgpr_write_b32 a82, v81\n\t"
        "v_accvgpr_write_b32 a83, v83\n\t"
        "v_accvgpr_write_b32 a84, v84\n\t"
        "v_accvgpr_write_b32 a85, v86\n\t"
        "v_accvgpr_write_b32 a86, v85\n\t"
        "v_accvgpr_write_b32 a87, v87\n\t"
        "v_accvgpr_write_b32 a88, v88\n\t"
        "v_accvgpr_write_b32 a89, v90\n\t"
        "v_accvgpr_write_b32 a90, v89\n\t"
        "v_accvgpr_write_b32 a91, v91\n\t"
        "v_accvgpr_write_b32 a92, v92\n\t"
        "v_accvgpr_write_b32 a93, v94\n\t"
        "v_accvgpr_write_b32 a94, v93\n\t"
        "v_accvgpr_write_b32 a95, v95\n\t"
        "v_accvgpr_write_b32 a96, v96\n\t"
        "v_accvgpr_write_b32 a97, v98\n\t"
        "v_accvgpr_write_b32 a98, v97\n\t"
        "v_accvgpr_write_b32 a99, v99\n\t"
        "v_accvgpr_write_b32 a100, v100\n\t"
        "v_accvgpr_write_b32 a101, v102\n\t"
        "v_accvgpr_write_b32 a102, v101\n\t"
        "v_accvgpr_write_b32 a103, v103\n\t"
        "v_accvgpr_write_b32 a104, v104\n\t"
        "v_accvgpr_write_b32 a105, v106\n\t"
        "v_accvgpr_write_b32 a106, v105\n\t"
        "v_accvgpr_write_b32 a107, v107\n\t"
        "v_accvgpr_write_b32 a108, v108\n\t"
        "v_accvgpr_write_b32 a109, v110\n\t"
        "v_accvgpr_write_b32 a110, v109\n\t"
        "v_accvgpr_write_b32 a111, v111\n\t"
        "s_mov_b32 s44, 1204224\n\t"
        "buffer_load_dwordx4 v[56:59], %[vw], %[rw], s44 offen offset:0\n\t"
        "buffer_load_dwordx4 v[60:63], %[vw], %[rw], s44 offen offset:1024\n\t"
        "buffer_load_dwordx4 v[64:67], %[vw], %[rw], s44 offen offset:2048\n\t"
        "buffer_load_dwordx4 v[68:71], %[vw], %[rw], s44 offen offset:3072\n\t"
        "s_mov_b32 s44, 1208320\n\t"
        "buffer_load_dwordx4 v[72:75], %[vw], %[rw], s44 offen offset:0\n\t"
        "buffer_load_dwordx4 v[76:79], %[vw], %[rw], s44 offen offset:1024\n\t"
        "buffer_load_dwordx4 v[80:83], %[vw], %[rw], s44 offen offset:2048\n\t"
        "buffer_load_dwordx4 v[84:87], %[vw], %[rw], s44 offen offset:3072\n\t"
        "s_mov_b32 s44, 1212416\n\t"
        "buffer_load_dwordx4 v[88:91], %[vw], %[rw], s44 offen offset:0\n\t"
        "buffer_load_dwordx4 v[92:95], %[vw], %[rw], s44 offen offset:1024\n\t"
        "buffer_load_dwordx4 v[96:99], %[vw], %[rw], s44 offen offset:2048\n\t"
        "buffer_load_dwordx4 v[100:103], %[vw], %[rw], s44 offen offset:3072\n\t"
        "s_mov_b32 s44, 1216512\n\t"
        "buffer_load_dwordx4 v[104:107], %[vw], %[rw], s44 offen offset:0\n\t"
        "buffer_load_dwordx4 v[108:111], %[vw], %[rw], s44 offen offset:1024\n\t"
        "s_waitcnt vmcnt(14)\n\t"
        "v_permlane16_swap_b32 v0, v2\n\t"
        "v_permlane16_swap_b32 v1, v3\n\t"
        "v_permlane16_swap_b32 v4, v6\n\t"
        "v_permlane16_swap_b32 v5, v7\n\t"
        "v_permlane16_swap_b32 v8, v10\n\t"
        "v_permlane16_swap_b32 v9, v11\n\t"
        "v_permlane16_swap_b32 v12, v14\n\t"
        "v_permlane16_swap_b32 v13, v15\n\t"
        "v_permlane16_swap_b32 v16, v18\n\t"
        "v_permlane16_swap_b32 v17, v19\n\t"
        "v_permlane16_swap_b32 v20, v22\n\t"
        "v_permlane16_swap_b32 v21, v23\n\t"
        "v_permlane16_swap_b32 v24, v26\n\t"
        "v_permlane16_swap_b32 v25, v27\n\t"
        "v_permlane16_swap_b32 v28, v30\n\t"
        "v_permlane16_swap_b32 v29, v31\n\t"
        "v_permlane16_swap_b32 v32, v34\n\t"
        "v_permlane16_swap_b32 v33, v35\n\t"
        "v_permlane16_swap_b32 v36, v38\n\t"
        "v_permlane16_swap_b32 v37, v39\n\t"
        "v_permlane16_swap_b32 v40, v42\n\t"
        "v_permlane16_swap_b32 v41, v43\n\t"
        "v_permlane16_swap_b32 v44, v46\n\t"
        "v_permlane16_swap_b32 v45, v47\n\t"
        "v_permlane16_swap_b32 v48, v50\n\t"
        "v_permlane16_swap_b32 v49, v51\n\t"
        "v_permlane16_swap_b32 v52, v54\n\t"
        "v_permlane16_swap_b32 v53, v55\n\t"
        "v_accvgpr_write_b32 a112, v0\n\t"
        "v_accvgpr_write_b32 a113, v2\n\t"
        "v_accvgpr_write_b32 a114, v1\n\t"
        "v_accvgpr_write_b32 a115, v3\n\t"
        "v_accvgpr_write_b32 a116, v4\n\t"
        "v_accvgpr_write_b32 a117, v6\n\t"
        "v_accvgpr_write_b32 a118, v5\n\t"
        "v_accvgpr_write_b32 a119, v7\n\t"
        "v_accvgpr_write_b32 a120, v8\n\t"
        "v_accvgpr_write_b32 a121, v10\n\t"
        "v_accvgpr_write_b32 a122, v9\n\t"
        "v_accvgpr_write_b32 a123, v11\n\t"
        "v_accvgpr_write_b32 a124, v12\n\t"
        "v_accvgpr_write_b32 a125, v14\n\t"
        "v_accvgpr_write_b32 a126, v13\n\t"
        "v_accvgpr_write_b32 a127, v15\n\t"
        "v_accvgpr_write_b32 a128, v16\n\t"
        "v_accvgpr_write_b32 a129, v18\n\t"
        "v_accvgpr_write_b32 a130, v17\n\t"
        "v_accvgpr_write_b32 a131, v19\n\t"
        "v_accvgpr_write_b32 a132, v20\n\t"
        "v_accvgpr_write_b32 a133, v22\n\t"
        "v_accvgpr_write_b32 a134, v21\n\t"
        "v_accvgpr_write_b32 a135, v23\n\t"
        "v_accvgpr_write_b32 a136, v24\n\t"
        "v_accvgpr_write_b32 a137, v26\n\t"
        "v_accvgpr_write_b32 a138, v25\n\t"
        "v_accvgpr_write_b32 a139, v27\n\t"
        "v_accvgpr_write_b32 a140, v28\n\t"
        "v_accvgpr_write_b32 a141, v30\n\t"
        "v_accvgpr_write_b32 a142, v29\n\t"
        "v_accvgpr_write_b32 a143, v31\n\t"
        "v_accvgpr_write_b32 a144, v32\n\t"
        "v_accvgpr_write_b32 a145, v34\n\t"
        "v_accvgpr_write_b32 a146, v33\n\t"
        "v_accvgpr_write_b32 a147, v35\n\t"
        "v_accvgpr_write_b32 a148, v36\n\t"
        "v_accvgpr_write_b32 a149, v38\n\t"
        "v_accvgpr_write_b32 a150, v37\n\t"
        "v_accvgpr_write_b32 a151, v39\n\t"
        "v_accvgpr_write_b32 a152, v40\n\t"
        "v_accvgpr_write_b32 a153, v42\n\t"
        "v_accvgpr_write_b32 a154, v41\n\t"
        "v_accvgpr_write_b32 a155, v43\n\t"
        "v_accvgpr_write_b32 a156, v44\n\t"
        "v_accvgpr_write_b32 a157, v46\n\t"
        "v_accvgpr_write_b32 a158, v45\n\t"
        "v_accvgpr_write_b32 a159, v47\n\t"
        "v_accvgpr_write_b32 a160, v48\n\t"
        "v_accvgpr_write_b32 a161, v50\n\t"
        "v_accvgpr_write_b32 a162, v49\n\t"
        "v_accvgpr_write_b32 a163, v51\n\t"
        "v_accvgpr_write_b32 a164, v52\n\t"
        "v_accvgpr_write_b32 a165, v54\n\t"
        "v_accvgpr_write_b32 a166, v53\n\t"
        "v_accvgpr_write_b32 a167, v55\n\t"
        "s_waitcnt vmcnt(0)\n\t"
        "v_permlane16_swap_b32 v56, v58\n\t"
        "v_permlane16_swap_b32 v57, v59\n\t"
        "v_permlane16_swap_b32 v60, v62\n\t"
        "v_permlane16_swap_b32 v61, v63\n\t"
        "v_permlane16_swap_b32 v64, v66\n\t"
        "v_permlane16_swap_b32 v65, v67\n\t"
        "v_permlane16_swap_b32 v68, v70\n\t"
        "v_permlane16_swap_b32 v69, v71\n\t"
        "v_permlane16_swap_b32 v72, v74\n\t"
        "v_permlane16_swap_b32 v73, v75\n\t"
        "v_permlane16_swap_b32 v76, v78\n\t"
        "v_permlane16_swap_b32 v77, v79\n\t"
        "v_permlane16_swap_b32 v80, v82\n\t"
        "v_permlane16_swap_b32 v81, v83\n\t"
        "v_permlane16_swap_b32 v84, v86\n\t"
        "v_permlane16_swap_b32 v85, v87\n\t"
        "v_permlane16_swap_b32 v88, v90\n\t"
        "v_permlane16_swap_b32 v89, v91\n\t"
        "v_permlane16_swap_b32 v92, v94\n\t"
        "v_permlane16_swap_b32 v93, v95\n\t"
        "v_permlane16_swap_b32 v96, v98\n\t"
        "v_permlane16_swap_b32 v97, v99\n\t"
        "v_permlane16_swap_b32 v100, v102\n\t"
        "v_permlane16_swap_b32 v101, v103\n\t"
        "v_permlane16_swap_b32 v104, v106\n\t"
        "v_permlane16_swap_b32 v105, v107\n\t"
        "v_permlane16_swap_b32 v108, v110\n\t"
        "v_permlane16_swap_b32 v109, v111\n\t"
        "v_accvgpr_write_b32 a168, v56\n\t"
        "v_accvgpr_write_b32 a169, v58\n\t"
        "v_accvgpr_write_b32 a170, v57\n\t"
        "v_accvgpr_write_b32 a171, v59\n\t"
        "v_accvgpr_write_b32 a172, v60\n\t"
        "v_accvgpr_write_b32 a173, v62\n\t"
        "v_accvgpr_write_b32 a174, v61\n\t"
        "v_accvgpr_write_b32 a175, v63\n\t"
        "v_accvgpr_write_b32 a176, v64\n\t"
        "v_accvgpr_write_b32 a177, v66\n\t"
        "v_accvgpr_write_b32 a178, v65\n\t"
        "v_accvgpr_write_b32 a179, v67\n\t"
        "v_accvgpr_write_b32 a180, v68\n\t"
        "v_accvgpr_write_b32 a181, v70\n\t"
        "v_accvgpr_write_b32 a182, v69\n\t"
        "v_accvgpr_write_b32 a183, v71\n\t"
        "v_accvgpr_write_b32 a184, v72\n\t"
        "v_accvgpr_write_b32 a185, v74\n\t"
        "v_accvgpr_write_b32 a186, v73\n\t"
        "v_accvgpr_write_b32 a187, v75\n\t"
        "v_accvgpr_write_b32 a188, v76\n\t"
        "v_accvgpr_write_b32 a189, v78\n\t"
        "v_accvgpr_write_b32 a190, v77\n\t"
        "v_accvgpr_write_b32 a191, v79\n\t"
        "v_accvgpr_write_b32 a192, v80\n\t"
        "v_accvgpr_write_b32 a193, v82\n\t"
        "v_accvgpr_write_b32 a194, v81\n\t"
        "v_accvgpr_write_b32 a195, v83\n\t"
        "v_accvgpr_write_b32 a196, v84\n\t"
        "v_accvgpr_write_b32 a197, v86\n\t"
        "v_accvgpr_write_b32 a198, v85\n\t"
        "v_accvgpr_write_b32 a199, v87\n\t"
        "v_accvgpr_write_b32 a200, v88\n\t"
        "v_accvgpr_write_b32 a201, v90\n\t"
        "v_accvgpr_write_b32 a202, v89\n\t"
        "v_accvgpr_write_b32 a203, v91\n\t"
        "v_accvgpr_write_b32 a204, v92\n\t"
        "v_accvgpr_write_b32 a205, v94\n\t"
        "v_accvgpr_write_b32 a206, v93\n\t"
        "v_accvgpr_write_b32 a207, v95\n\t"
        "v_accvgpr_write_b32 a208, v96\n\t"
        "v_accvgpr_write_b32 a209, v98\n\t"
        "v_accvgpr_write_b32 a210, v97\n\t"
        "v_accvgpr_write_b32 a211, v99\n\t"
        "v_accvgpr_write_b32 a212, v100\n\t"
        "v_accvgpr_write_b32 a213, v102\n\t"
        "v_accvgpr_write_b32 a214, v101\n\t"
        "v_accvgpr_write_b32 a215, v103\n\t"
        "v_accvgpr_write_b32 a216, v104\n\t"
        "v_accvgpr_write_b32 a217, v106\n\t"
        "v_accvgpr_write_b32 a218, v105\n\t"
        "v_accvgpr_write_b32 a219, v107\n\t"
        "v_accvgpr_write_b32 a220, v108\n\t"
        "v_accvgpr_write_b32 a221, v110\n\t"
        "v_accvgpr_write_b32 a222, v109\n\t"
        "v_accvgpr_write_b32 a223, v111\n\t"
        "s_mov_b32 s50, 0xff00ff\n\t"
        "s_mov_b32 s51, 0xff00ff\n\t"
        "s_mov_b32 s52, 0x0\n\t"
        "s_mov_b32 s53, 0xffffffff\n\t"
        "s_mov_b32 s40, 0\n\t"
        "s_mov_b32 s43, %[bud]\n\t"
        "s_mov_b32 s55, 0\n\t"
        "s_mov_b64 s[46:47], 0\n\t"
        "s_mov_b32 s77, %[t0]\n\t"
        "v_mbcnt_lo_u32_b32 v0, -1, 0\n\t"
        "v_mbcnt_hi_u32_b32 v0, -1, v0\n\t"
        "v_lshlrev_b32 v0, 2, v0\n\t"
        "s_mov_b32 s82, 132\n\t"
        "s_mov_b32 s83, 62\n\t"
        "LPA_%=:\n\t"
        "buffer_load_dword v2, off, %[rw], %[fr] offset:1536 sc1\n\t"
        "s_waitcnt vmcnt(0)\n\t"
        "v_cmp_eq_u32 vcc, 0, v2\n\t"
        "s_cbranch_vccz LPB_%=\n\t"
        "s_sleep 8\n\t"
        "s_sub_u32 s43, s43, 1\n\t"
        "s_cbranch_scc0 LPA_%=\n\t"
        "s_mov_b32 s43, 0\n\t"
        "LPB_%=:\n\t"
        "buffer_load_dword v2, v0, %[rw], %[fr] offen sc1\n\t"
        "s_waitcnt vmcnt(0)\n\t"
        "v_cmp_eq_u32_e64 s[80:81], 2, v2\n\t"
        "LFp_%=:\n\t"
        "s_bitcmp1_b64 s[46:47], s40\n\t"
        "s_cbranch_scc1 LGp_%=\n\t"
        "buffer_load_dword v0, %[vl4], %[rw], %[fo] offen sc1\n\t"
        "s_waitcnt vmcnt(0)\n\t"
        "v_cmp_ne_u32_e64 s[46:47], 0, v0\n\t"
        "s_sub_u32 s43, s43, 1\n\t"
        "s_cbranch_scc1 LDp_%=\n\t"
        "s_sleep 1\n\t"
        "s_branch LFp_%=\n\t"
        "LDp_%=:\n\t"
        "s_mov_b32 s43, 0\n\t"
        "LGp_%=:\n\t"
        "s_bitcmp1_b64 s[80:81], s40\n\t"
        "s_cselect_b32 s44, 8192, 0\n\t"
        "s_lshl_b32 s41, s40, 18\n\t"
        "s_add_u32 s41, s41, s44\n\t"
        "buffer_load_dwordx4 v[112:115], %[vv], %[rw], s55 offen sc1\n\t"
        "buffer_load_dwordx4 v[116:119], %[vv], %[rw], s55 offen offset:1024 sc1\n\t"
        "buffer_load_dwordx4 v[120:123], %[vv], %[rw], s55 offen offset:2048 sc1\n\t"
        "buffer_load_dwordx4 v[124:127], %[vv], %[rw], s55 offen offset:3072 sc1\n\t"
        "buffer_load_dword v109, %[vl4], %[rw], %[fo] offen sc1\n\t"
        "s_waitcnt vmcnt(0)\n\t"
        "s_add_u32 s56, s41, %[c0]\n\t"
        "LOOP_%=:\n\t"
        "s_add_u32 s42, s77, 0\n\t"
        "LUm_%=:\n\t"
        "s_memrealtime s[78:79]\n\t"
        "s_waitcnt lgkmcnt(0)\n\t"
        "s_sub_u32 s78, s78, s42\n\t"
        "s_cmp_lt_i32 s78, 0\n\t"
        "s_cbranch_scc1 LUm_%=\n\t"
        "s_cmp_ge_u32 s78, 2\n\t"
        "s_cbranch_scc1 LVm_%=\n\t"
        "s_nop 7\n\t"
        "s_nop 6\n\t"
        "LVm_%=:\n\t"
        "s_bitcmp1_b32 s78, 0\n\t"
        "s_cbranch_scc1 LWm_%=\n\t"
        "s_nop 7\n\t"
        "s_nop 0\n\t"
        "LWm_%=:\n\t"
        "buffer_load_dwordx4 v[0:3], %[om0], %[rh], s56 offen sc1\n\t"
        "buffer_load_dwordx4 v[4:7], %[gm1], %[rh], s56 offen sc1\n\t"
        "v_mov_b32 v36, 0\n\t"
        "v_mov_b32 v37, 0\n\t"
        "v_mov_b32 v38, 0\n\t"
        "v_mov_b32 v39, 0\n\t"
        "v_mov_b32 v40, 0\n\t"
        "v_mov_b32 v41, 0\n\t"
        "v_mov_b32 v42, 0\n\t"
        "v_mov_b32 v43, 0\n\t"
        "v_mov_b32 v44, 0\n\t"
        "v_mov_b32 v45, 0\n\t"
        "v_mov_b32 v46, 0\n\t"
        "v_mov_b32 v47, 0\n\t"
        "v_mov_b32 v48, 0\n\t"
        "v_mov_b32 v49, 0\n\t"
        "v_mov_b32 v50, 0\n\t"
        "v_mov_b32 v51, 0\n\t"
        "s_waitcnt vmcnt(4)\n\t"
        "v_cmp_ne_u32_e64 s[46:47], 0, v109\n\t"
        "v_mov_b32 v60, v112\n\t"
        "v_mov_b32 v61, v113\n\t"
        "v_mov_b32 v62, v114\n\t"
        "v_mov_b32 v63, v115\n\t"
        "v_mov_b32 v64, v116\n\t"
        "v_mov_b32 v65, v117\n\t"
        "v_mov_b32 v66, v118\n\t"
        "v_mov_b32 v67, v119\n\t"
        "v_mov_b32 v68, v120\n\t"
        "v_mov_b32 v69, v121\n\t"
        "v_mov_b32 v70, v122\n\t"
        "v_mov_b32 v71, v123\n\t"
        "v_mov_b32 v72, v124\n\t"
        "v_mov_b32 v73, v125\n\t"
        "v_mov_b32 v74, v126\n\t"
        "v_mov_b32 v75, v127\n\t"
        "s_add_u32 s45, s40, 1\n\t"
        "s_cmp_ge_u32 s40, 62\n\t"
        "s_cselect_b32 s44, 0, 458752\n\t"
        "s_min_u32 s48, s45, 62\n\t"
        "s_add_u32 s55, s55, s44\n\t"
        "s_waitcnt vmcnt(0)\n\t"
        "v_max3_u32 v96, v0, v1, v2\n\t"
        "v_max3_u32 v96, v96, v3, v4\n\t"
        "v_max3_u32 v96, v96, v5, v6\n\t"
        "v_max_u32 v96, v96, v7\n\t"
        "v_cmp_eq_u32 vcc, -1, v96\n\t"
        "s_cbranch_vccnz LRm_%=\n\t"
        "LGOm_%=:\n\t"
        "ds_write_b128 %[om0], v[0:3] offset:4096\n\t"
        "ds_write_b128 %[om1], v[4:7] offset:4096\n\t"
        "s_waitcnt lgkmcnt(0)\n\t"
        "s_barrier\n\t"
        "ds_read_b32 v108, %[lts]\n\t"
        "ds_read_b128 v[8:11], %[lrd] offset:0\n\t"
        "ds_read_b128 v[12:15], %[lrd] offset:1024\n\t"
        "ds_read_b128 v[16:19], %[lrd] offset:2048\n\t"
        "ds_read_b128 v[20:23], %[lrd] offset:3072\n\t"
        "ds_read_b128 v[24:27], %[lrd] offset:4096\n\t"
        "ds_read_b128 v[28:31], %[lrd] offset:5120\n\t"
        "ds_read_b128 v[32:35], %[lrd] offset:6144\n\t"
        "s_mov_b32 s49, s41\n\t"
        "s_waitcnt lgkmcnt(6)\n\t"
        "v_smfmac_f32_16x16x64_bf16 v[36:39], v[8:11], a[0:7], %[idx]\n\t"
        "v_smfmac_f32_16x16x64_bf16 v[40:43], v[8:11], a[56:63], %[idx]\n\t"
        "v_smfmac_f32_16x16x64_bf16 v[44:47], v[8:11], a[112:119], %[idx]\n\t"
        "v_smfmac_f32_16x16x64_bf16 v[48:51], v[8:11], a[168:175], %[idx]\n\t"
        "s_waitcnt lgkmcnt(5)\n\t"
        "v_smfmac_f32_16x16x64_bf16 v[36:39], v[12:15], a[8:15], %[idx]\n\t"
        "v_smfmac_f32_16x16x64_bf16 v[40:43], v[12:15], a[64:71], %[idx]\n\t"
        "v_smfmac_f32_16x16x64_bf16 v[44:47], v[12:15], a[120:127], %[idx]\n\t"
        "v_smfmac_f32_16x16x64_bf16 v[48:51], v[12:15], a[176:183], %[idx]\n\t"
        "s_add_u32 s56, s41, %[c0]\n\t"
        "LTA_%=:\n\t"
        "v_max_u32_dpp v108, v108, v108 row_shr:1 row_mask:0xf bank_mask:0xf bound_ctrl:1\n\t"
        "s_nop 1\n\t"
        "v_max_u32_dpp v108, v108, v108 row_shr:2 row_mask:0xf bank_mask:0xf bound_ctrl:1\n\t"
        "s_nop 1\n\t"
        "v_max_u32_dpp v108, v108, v108 row_shr:4 row_mask:0xf bank_mask:0xf bound_ctrl:1\n\t"
        "s_nop 1\n\t"
        "v_max_u32_dpp v108, v108, v108 row_shr:8 row_mask:0xf bank_mask:0xf bound_ctrl:1\n\t"
        "s_nop 0\n\t"
        "v_readlane_b32 s44, v108, 15\n\t"
        "v_readlane_b32 s42, v108, 31\n\t"
        "s_nop 1\n\t"
        "s_max_u32 s44, s44, s42\n\t"
        "s_add_u32 s77, s77, s44\n\t"
        "s_cmp_ge_u32 s40, 6\n\t"
        "s_cselect_b32 s42, s44, 0\n\t"
        "s_cmp_lg_u32 s42, 0\n\t"
        "s_cselect_b32 s42, 2, 0\n\t"
        "s_cselect_b32 s57, 1, 0\n\t"
        "s_add_u32 s83, s83, s57\n\t"
        "s_min_u32 s83, s83, 64\n\t"
        "s_add_u32 s82, s82, s42\n\t"
        "s_min_u32 s82, s82, 136\n\t"
        "LTB_%=:\n\t"
        "s_waitcnt lgkmcnt(4)\n\t"
        "v_smfmac_f32_16x16x64_bf16 v[36:39], v[16:19], a[16:23], %[idx]\n\t"
        "v_smfmac_f32_16x16x64_bf16 v[40:43], v[16:19], a[72:79], %[idx]\n\t"
        "v_smfmac_f32_16x16x64_bf16 v[44:47], v[16:19], a[128:135], %[idx]\n\t"
        "v_smfmac_f32_16x16x64_bf16 v[48:51], v[16:19], a[184:191], %[idx]\n\t"
        "s_bitcmp1_b64 s[80:81], s45\n\t"
        "s_cselect_b32 s44, 8192, 0\n\t"
        "s_lshl_b32 s41, s45, 18\n\t"
        "s_add_u32 s41, s41, s44\n\t"
        "s_add_u32 s42, s77, s83\n\t"
        "LUs_%=:\n\t"
        "s_memrealtime s[78:79]\n\t"
        "s_waitcnt lgkmcnt(0)\n\t"
        "s_sub_u32 s78, s78, s42\n\t"
        "s_cmp_lt_i32 s78, 0\n\t"
        "s_cbranch_scc1 LUs_%=\n\t"
        "s_cmp_ge_u32 s78, 2\n\t"
        "s_cbranch_scc1 LVs_%=\n\t"
        "s_nop 7\n\t"
        "s_nop 6\n\t"
        "LVs_%=:\n\t"
        "s_bitcmp1_b32 s78, 0\n\t"
        "s_cbranch_scc1 LWs_%=\n\t"
        "s_nop 7\n\t"
        "s_nop 0\n\t"
        "LWs_%=:\n\t"
        "buffer_load_dwordx4 v[168:171], %[oe], %[rh], s56 offen sc1\n\t"
        "buffer_load_dwordx4 v[172:175], %[oe], %[rh], s56 offen offset:1024 sc1\n\t"
        "s_waitcnt lgkmcnt(3)\n\t"
        "v_smfmac_f32_16x16x64_bf16 v[36:39], v[20:23], a[24:31], %[idx]\n\t"
        "v_smfmac_f32_16x16x64_bf16 v[40:43], v[20:23], a[80:87], %[idx]\n\t"
        "v_smfmac_f32_16x16x64_bf16 v[44:47], v[20:23], a[136:143], %[idx]\n\t"
        "v_smfmac_f32_16x16x64_bf16 v[48:51], v[20:23], a[192:199], %[idx]\n\t"
        "s_waitcnt lgkmcnt(2)\n\t"
        "v_smfmac_f32_16x16x64_bf16 v[36:39], v[24:27], a[32:39], %[idx]\n\t"
        "v_smfmac_f32_16x16x64_bf16 v[40:43], v[24:27], a[88:95], %[idx]\n\t"
        "buffer_load_dwordx4 v[160:163], %[osp], %[rh], s56 offen sc1\n\t"
        "buffer_load_dwordx4 v[164:167], %[osp], %[rh], s56 offen offset:512 sc1\n\t"
        "buffer_load_dwordx2 v[176:177], %[ost], %[rh], s56 offen sc1\n\t"
        "v_smfmac_f32_16x16x64_bf16 v[44:47], v[24:27], a[144:151], %[idx]\n\t"
        "v_smfmac_f32_16x16x64_bf16 v[48:51], v[24:27], a[200:207], %[idx]\n\t"
        "s_waitcnt lgkmcnt(1)\n\t"
        "v_smfmac_f32_16x16x64_bf16 v[36:39], v[28:31], a[40:47], %[idx]\n\t"
        "v_smfmac_f32_16x16x64_bf16 v[40:43], v[28:31], a[96:103], %[idx]\n\t"
        "LFq_%=:\n\t"
        "s_bitcmp1_b64 s[46:47], s48\n\t"
        "s_cbranch_scc1 LGq_%=\n\t"
        "buffer_load_dword v106, %[vl4], %[rw], %[fo] offen sc1\n\t"
        "s_waitcnt vmcnt(0)\n\t"
        "v_cmp_ne_u32_e64 s[46:47], 0, v106\n\t"
        "s_sub_u32 s43, s43, 1\n\t"
        "s_cbranch_scc1 LDq_%=\n\t"
        "s_sleep 1\n\t"
        "s_branch LFq_%=\n\t"
        "LDq_%=:\n\t"
        "s_mov_b32 s43, 0\n\t"
        "LGq_%=:\n\t"
        "buffer_load_dwordx4 v[112:115], %[vv], %[rw], s55 offen sc1\n\t"
        "v_smfmac_f32_16x16x64_bf16 v[44:47], v[28:31], a[152:159], %[idx]\n\t"
        "buffer_load_dwordx4 v[116:119], %[vv], %[rw], s55 offen offset:1024 sc1\n\t"
        "v_smfmac_f32_16x16x64_bf16 v[48:51], v[28:31], a[208:215], %[idx]\n\t"
        "buffer_load_dwordx4 v[120:123], %[vv], %[rw], s55 offen offset:2048 sc1\n\t"
        "s_waitcnt lgkmcnt(0)\n\t"
        "v_smfmac_f32_16x16x64_bf16 v[36:39], v[32:35], a[48:55], %[idx]\n\t"
        "buffer_load_dwordx4 v[124:127], %[vv], %[rw], s55 offen offset:3072 sc1\n\t"
        "buffer_load_dword v109, %[vl4], %[rw], %[fo] offen sc1\n\t"
        "v_smfmac_f32_16x16x64_bf16 v[40:43], v[32:35], a[104:111], %[idx]\n\t"
        "v_smfmac_f32_16x16x64_bf16 v[44:47], v[32:35], a[160:167], %[idx]\n\t"
        "v_smfmac_f32_16x16x64_bf16 v[48:51], v[32:35], a[216:223], %[idx]\n\t"
        "s_nop 5\n\t"
        "v_permlane32_swap_b32 v36, v40\n\t"
        "v_permlane32_swap_b32 v37, v41\n\t"
        "v_permlane32_swap_b32 v38, v42\n\t"
        "v_permlane32_swap_b32 v39, v43\n\t"
        "v_pk_add_f32 v[96:97], v[36:37], v[40:41]\n\t"
        "v_pk_add_f32 v[98:99], v[38:39], v[42:43]\n\t"
        "v_cvt_pk_bf16_f32 v52, v96, v97\n\t"
        "v_cvt_pk_bf16_f32 v53, v98, v99\n\t"
        "v_permlane32_swap_b32 v44, v48\n\t"
        "v_permlane32_swap_b32 v45, v49\n\t"
        "v_permlane32_swap_b32 v46, v50\n\t"
        "v_permlane32_swap_b32 v47, v51\n\t"
        "v_pk_add_f32 v[96:97], v[44:45], v[48:49]\n\t"
        "v_pk_add_f32 v[98:99], v[46:47], v[50:51]\n\t"
        "v_cvt_pk_bf16_f32 v56, v96, v97\n\t"
        "v_cvt_pk_bf16_f32 v57, v98, v99\n\t"
        "s_add_u32 s77, s77, s82\n\t"
        "s_sub_u32 s42, s77, 22\n\t"
        "s_waitcnt vmcnt(9)\n\t"
        "v_mfma_f32_16x16x32_bf16 v[36:39], v[168:171], v[60:63], 0\n\t"
        "v_mfma_f32_16x16x32_bf16 v[40:43], v[168:171], v[64:67], 0\n\t"
        "s_waitcnt vmcnt(8)\n\t"
        "v_mfma_f32_16x16x32_bf16 v[44:47], v[172:175], v[68:71], 0\n\t"
        "v_mfma_f32_16x16x32_bf16 v[48:51], v[172:175], v[72:75], 0\n\t"
        "s_nop 4\n\t"
        "v_cndmask_b32_e64 v100, v36, v40, s[52:53]\n\t"
        "v_cndmask_b32_e64 v101, v37, v41, s[52:53]\n\t"
        "v_cndmask_b32_e64 v102, v38, v42, s[52:53]\n\t"
        "v_cndmask_b32_e64 v103, v39, v43, s[52:53]\n\t"
        "v_cvt_pk_bf16_f32 v54, v100, v101\n\t"
        "v_cvt_pk_bf16_f32 v55, v102, v103\n\t"
        "v_cndmask_b32_e64 v104, v44, v48, s[52:53]\n\t"
        "v_cndmask_b32_e64 v105, v45, v49, s[52:53]\n\t"
        "v_cndmask_b32_e64 v106, v46, v50, s[52:53]\n\t"
        "v_cndmask_b32_e64 v107, v47, v51, s[52:53]\n\t"
        "v_cvt_pk_bf16_f32 v58, v104, v105\n\t"
        "v_cvt_pk_bf16_f32 v59, v106, v107\n\t"
        "s_waitcnt vmcnt(7)\n\t"
        "v_mfma_f32_16x16x32_bf16 v[76:79], %[sel0], v[160:163], 0\n\t"
        "s_waitcnt vmcnt(6)\n\t"
        "v_mfma_f32_16x16x32_bf16 v[76:79], %[sel1], v[164:167], v[76:79]\n\t"
        "s_memrealtime s[78:79]\n\t"
        "s_waitcnt vmcnt(5)\n\t"
        "s_nop 5\n\t"
        "v_max3_f32 v96, v76, v77, v176\n\t"
        "v_max3_f32 v97, v96, v78, v79\n\t"
        "v_max3_f32 v96, v96, v78, v79\n\t"
        "s_nop 1\n\t"
        "v_permlane16_swap_b32 v96, v97\n\t"
        "v_max_f32 v96, v96, v97\n\t"
        "v_pk_add_f32 v[76:77], v[76:77], v[96:97] op_sel_hi:[1,0] neg_lo:[0,1] neg_hi:[0,1]\n\t"
        "v_pk_add_f32 v[78:79], v[78:79], v[96:97] op_sel_hi:[1,0] neg_lo:[0,1] neg_hi:[0,1]\n\t"
        "v_exp_f32 v76, v76\n\t"
        "v_exp_f32 v77, v77\n\t"
        "v_sub_f32 v98, v176, v96\n\t"
        "v_exp_f32 v78, v78\n\t"
        "v_exp_f32 v79, v79\n\t"
        "v_add_f32 v96, v76, v77\n\t"
        "v_exp_f32 v98, v98\n\t"
        "v_add_f32 v96, v96, v78\n\t"
        "v_add_f32 v97, v96, v79\n\t"
        "v_add_f32 v96, v96, v79\n\t"
        "v_mul_f32 v99, v98, v177\n\t"
        "s_nop 0\n\t"
        "v_permlane16_swap_b32 v96, v97\n\t"
        "v_add_f32 v99, v99, v96\n\t"
        "v_add_f32 v96, v99, v97\n\t"
        "v_rcp_f32 v96, v96\n\t"
        "s_nop 0\n\t"
        "v_mul_f32 v98, v98, v96\n\t"
        "v_pk_mul_f32 v[76:77], v[76:77], v[96:97] op_sel_hi:[1,0]\n\t"
        "v_pk_mul_f32 v[78:79], v[78:79], v[96:97] op_sel_hi:[1,0]\n\t"
        "v_cvt_pk_bf16_f32 v98, v98, v98\n\t"
        "v_cvt_pk_bf16_f32 v80, v76, v77\n\t"
        "v_cvt_pk_bf16_f32 v81, v78, v79\n\t"
        "v_and_b32 v82, v98, %[mz]\n\t"
        "v_and_b32 v83, v98, %[mw]\n\t"
        "v_mov_b32_dpp v84, v80 row_ror:8 row_mask:0xf bank_mask:0xf bound_ctrl:1\n\t"
        "v_mov_b32_dpp v85, v81 row_ror:8 row_mask:0xf bank_mask:0xf bound_ctrl:1\n\t"
        "v_mfma_f32_16x16x32_bf16 v[88:91], v[52:55], v[80:83], 0\n\t"
        "v_mov_b32_dpp v86, v82 row_ror:8 row_mask:0xf bank_mask:0xf bound_ctrl:1\n\t"
        "v_mov_b32_dpp v87, v83 row_ror:8 row_mask:0xf bank_mask:0xf bound_ctrl:1\n\t"
        "s_waitcnt lgkmcnt(0)\n\t"
        "s_sub_u32 s78, s78, s42\n\t"
        "v_mfma_f32_16x16x32_bf16 v[88:91], v[56:59], v[84:87], v[88:91]\n\t"
        "s_add_u32 s44, s78, 40\n\t"
        "s_cmp_gt_i32 s78, 20\n\t"
        "s_cselect_b32 s44, s44, 40\n\t"
        "s_cmp_gt_i32 s78, 0\n\t"
        "s_cselect_b32 s44, s44, 0\n\t"
        "v_mov_b32 v96, s44\n\t"
        "ds_write_b32 %[lsw], v96\n\t"
        "s_mov_b64 exec, s[50:51]\n\t"
        "ds_write_b128 %[lpw], v[88:91]\n\t"
        "v_cmp_u_f32 vcc, v88, v88\n\t"
        "s_mov_b64 exec, -1\n\t"
        "s_cbranch_vccnz LSR_%=\n\t"
        "LSG_%=:\n\t"
        "s_waitcnt lgkmcnt(0)\n\t"
        "s_barrier\n\t"
        "ds_read2st64_b64 v[88:91], %[l8] offset0:0 offset1:1\n\t"
        "ds_read2st64_b64 v[92:95], %[l8] offset0:2 offset1:3\n\t"
        "ds_read_b128 v[100:103], %[lsr]\n\t"
        "s_add_u32 s56, s41, %[c0]\n\t"
        "s_mov_b32 s40, s45\n\t"
        "s_waitcnt lgkmcnt(2)\n\t"
        "v_pk_add_f32 v[88:89], v[88:89], v[90:91]\n\t"
        "s_waitcnt lgkmcnt(1)\n\t"
        "v_pk_add_f32 v[92:93], v[92:93], v[94:95]\n\t"
        "v_pk_add_f32 v[88:89], v[88:89], v[92:93]\n\t"
        "v_cvt_pk_bf16_f32 v96, v88, v89\n\t"
        "v_min_u32 v96, -2, v96\n\t"
        "buffer_store_dword v96, %[opub], %[rh], s41 offen sc1\n\t"
        "s_waitcnt lgkmcnt(0)\n\t"
        "v_max3_u32 v100, v100, v101, v102\n\t"
        "s_mov_b64 exec, 0xffff\n\t"
        "v_max_u32 v100, v100, v103\n\t"
        "buffer_store_dword v100, %[opt], %[rh], s41 offen sc1\n\t"
        "s_mov_b64 exec, -1\n\t"
        "s_cmp_lt_u32 s40, 63\n\t"
        "s_cbranch_scc1 LOOP_%=\n\t"
        "s_branch LFIN_%=\n\t"
        "LSR_%=:\n\t"
        "s_mov_b32 s59, %[c1]\n\t"
        "s_mov_b32 s58, %[c2]\n\t"
        "LSP_%=:\n\t"
        "v_max3_u32 v96, v160, v161, v162\n\t"
        "v_max3_u32 v96, v96, v163, v164\n\t"
        "v_max3_u32 v96, v96, v165, v166\n\t"
        "v_max3_u32 v96, v96, v167, v168\n\t"
        "v_max3_u32 v96, v96, v169, v170\n\t"
        "v_max3_u32 v96, v96, v171, v172\n\t"
        "v_max3_u32 v96, v96, v173, v174\n\t"
        "v_max3_u32 v96, v96, v175, v176\n\t"
        "v_max_u32 v96, v96, v177\n\t"
        "v_cmp_eq_u32 vcc, -1, v96\n\t"
        "s_cbranch_vccz LSD_%=\n\t"
        "s_mov_b32 s57, s58\n\t"
        "s_mov_b32 s58, %[c3]\n\t"
        "s_add_u32 s56, s49, s59\n\t"
        "s_mov_b32 s59, s57\n\t"
        "buffer_load_dwordx4 v[168:171], %[oe], %[rh], s56 offen sc1\n\t"
        "buffer_load_dwordx4 v[172:175], %[oe], %[rh], s56 offen offset:1024 sc1\n\t"
        "buffer_load_dwordx4 v[160:163], %[osp], %[rh], s56 offen sc1\n\t"
        "buffer_load_dwordx4 v[164:167], %[osp], %[rh], s56 offen offset:512 sc1\n\t"
        "buffer_load_dwordx2 v[176:177], %[ost], %[rh], s56 offen sc1\n\t"
        "s_waitcnt vmcnt(0)\n\t"
        "s_sub_u32 s43, s43, 1\n\t"
        "s_cbranch_scc0 LSP_%=\n\t"
        "s_mov_b32 s43, 0\n\t"
        "LSD_%=:\n\t"
        "s_waitcnt vmcnt(4)\n\t"
        "v_mfma_f32_16x16x32_bf16 v[36:39], v[168:171], v[60:63], 0\n\t"
        "v_mfma_f32_16x16x32_bf16 v[40:43], v[168:171], v[64:67], 0\n\t"
        "s_waitcnt vmcnt(3)\n\t"
        "v_mfma_f32_16x16x32_bf16 v[44:47], v[172:175], v[68:71], 0\n\t"
        "v_mfma_f32_16x16x32_bf16 v[48:51], v[172:175], v[72:75], 0\n\t"
        "s_nop 4\n\t"
        "v_cndmask_b32_e64 v100, v36, v40, s[52:53]\n\t"
        "v_cndmask_b32_e64 v101, v37, v41, s[52:53]\n\t"
        "v_cndmask_b32_e64 v102, v38, v42, s[52:53]\n\t"
        "v_cndmask_b32_e64 v103, v39, v43, s[52:53]\n\t"
        "v_cvt_pk_bf16_f32 v54, v100, v101\n\t"
        "v_cvt_pk_bf16_f32 v55, v102, v103\n\t"
        "v_cndmask_b32_e64 v104, v44, v48, s[52:53]\n\t"
        "v_cndmask_b32_e64 v105, v45, v49, s[52:53]\n\t"
        "v_cndmask_b32_e64 v106, v46, v50, s[52:53]\n\t"
        "v_cndmask_b32_e64 v107, v47, v51, s[52:53]\n\t"
        "v_cvt_pk_bf16_f32 v58, v104, v105\n\t"
        "v_cvt_pk_bf16_f32 v59, v106, v107\n\t"
        "s_waitcnt vmcnt(2)\n\t"
        "v_mfma_f32_16x16x32_bf16 v[76:79], %[sel0], v[160:163], 0\n\t"
        "s_waitcnt vmcnt(1)\n\t"
        "v_mfma_f32_16x16x32_bf16 v[76:79], %[sel1], v[164:167], v[76:79]\n\t"
        "s_memrealtime s[78:79]\n\t"
        "s_waitcnt vmcnt(0)\n\t"
        "s_nop 5\n\t"
        "v_max3_f32 v96, v76, v77, v176\n\t"
        "v_max3_f32 v97, v96, v78, v79\n\t"
        "v_max3_f32 v96, v96, v78, v79\n\t"
        "s_nop 1\n\t"
        "v_permlane16_swap_b32 v96, v97\n\t"
        "v_max_f32 v96, v96, v97\n\t"
        "v_pk_add_f32 v[76:77], v[76:77], v[96:97] op_sel_hi:[1,0] neg_lo:[0,1] neg_hi:[0,1]\n\t"
        "v_pk_add_f32 v[78:79], v[78:79], v[96:97] op_sel_hi:[1,0] neg_lo:[0,1] neg_hi:[0,1]\n\t"
        "v_exp_f32 v76, v76\n\t"
        "v_exp_f32 v77, v77\n\t"
        "v_sub_f32 v98, v176, v96\n\t"
        "v_exp_f32 v78, v78\n\t"
        "v_exp_f32 v79, v79\n\t"
        "v_add_f32 v96, v76, v77\n\t"
        "v_exp_f32 v98, v98\n\t"
        "v_add_f32 v96, v96, v78\n\t"
        "v_add_f32 v97, v96, v79\n\t"
        "v_add_f32 v96, v96, v79\n\t"
        "v_mul_f32 v99, v98, v177\n\t"
        "s_nop 0\n\t"
        "v_permlane16_swap_b32 v96, v97\n\t"
        "v_add_f32 v99, v99, v96\n\t"
        "v_add_f32 v96, v99, v97\n\t"
        "v_rcp_f32 v96, v96\n\t"
        "s_nop 0\n\t"
        "v_mul_f32 v98, v98, v96\n\t"
        "v_pk_mul_f32 v[76:77], v[76:77], v[96:97] op_sel_hi:[1,0]\n\t"
        "v_pk_mul_f32 v[78:79], v[78:79], v[96:97] op_sel_hi:[1,0]\n\t"
        "v_cvt_pk_bf16_f32 v98, v98, v98\n\t"
        "v_cvt_pk_bf16_f32 v80, v76, v77\n\t"
        "v_cvt_pk_bf16_f32 v81, v78, v79\n\t"
        "v_and_b32 v82, v98, %[mz]\n\t"
        "v_and_b32 v83, v98, %[mw]\n\t"
        "v_mov_b32_dpp v84, v80 row_ror:8 row_mask:0xf bank_mask:0xf bound_ctrl:1\n\t"
        "v_mov_b32_dpp v85, v81 row_ror:8 row_mask:0xf bank_mask:0xf bound_ctrl:1\n\t"
        "v_mfma_f32_16x16x32_bf16 v[88:91], v[52:55], v[80:83], 0\n\t"
        "v_mov_b32_dpp v86, v82 row_ror:8 row_mask:0xf bank_mask:0xf bound_ctrl:1\n\t"
        "v_mov_b32_dpp v87, v83 row_ror:8 row_mask:0xf bank_mask:0xf bound_ctrl:1\n\t"
        "s_waitcnt lgkmcnt(0)\n\t"
        "s_sub_u32 s78, s78, s42\n\t"
        "v_mfma_f32_16x16x32_bf16 v[88:91], v[56:59], v[84:87], v[88:91]\n\t"
        "s_add_u32 s44, s78, 40\n\t"
        "s_cmp_gt_i32 s78, 20\n\t"
        "s_cselect_b32 s44, s44, 40\n\t"
        "s_cmp_gt_i32 s78, 0\n\t"
        "s_cselect_b32 s44, s44, 0\n\t"
        "v_mov_b32 v96, s44\n\t"
        "ds_write_b32 %[lsw], v96\n\t"
        "s_mov_b64 exec, s[50:51]\n\t"
        "ds_write_b128 %[lpw], v[88:91]\n\t"
        "v_cmp_u_f32 vcc, v88, v88\n\t"
        "s_mov_b64 exec, -1\n\t"
        "s_branch LSG_%=\n\t"
        "LFIN_%=:\n\t"
        "s_waitcnt vmcnt(0)\n\t"
        "s_branch LOUT_%=\n\t"
        "LRm_%=:\n\t"
        "s_mov_b32 s59, %[c1]\n\t"
        "s_mov_b32 s58, %[c2]\n\t"
        "LPm_%=:\n\t"
        "s_mov_b32 s57, s58\n\t"
        "s_mov_b32 s58, %[c3]\n\t"
        "s_add_u32 s56, s41, s59\n\t"
        "s_mov_b32 s59, s57\n\t"
        "buffer_load_dwordx4 v[0:3], %[om0], %[rh], s56 offen sc1\n\t"
        "buffer_load_dwordx4 v[4:7], %[gm1], %[rh], s56 offen sc1\n\t"
        "s_waitcnt vmcnt(0)\n\t"
        "v_max3_u32 v96, v0, v1, v2\n\t"
        "v_max3_u32 v96, v96, v3, v4\n\t"
        "v_max3_u32 v96, v96, v5, v6\n\t"
        "v_max_u32 v96, v96, v7\n\t"
        "v_cmp_eq_u32 vcc, -1, v96\n\t"
        "s_cbranch_vccz LKm_%=\n\t"
        "s_sub_u32 s43, s43, 1\n\t"
        "s_cbranch_scc0 LPm_%=\n\t"
        "s_mov_b32 s43, 0\n\t"
        "v_mov_b32 v0, 0\n\t"
        "v_mov_b32 v1, 0\n\t"
        "v_mov_b32 v2, 0\n\t"
        "v_mov_b32 v3, 0\n\t"
        "v_mov_b32 v4, 0\n\t"
        "v_mov_b32 v5, 0\n\t"
        "v_mov_b32 v6, 0\n\t"
        "v_mov_b32 v7, 0\n\t"
        "LKm_%=:\n\t"
        "s_nop 1\n\t"
        "s_branch LGOm_%=\n\t"
        "LOUT_%=:\n\t"
        :: VDBG [rw] "s"(rw), [rh] "s"(rh), [fr] "s"((unsigned)O_FLG + F_R), [bud] "s"(SPIN_LIMIT), RUNGS, [fo] "s"((unsigned)O_FLG + F_X + xc * 256), [vl4] "v"(lane * 4), [vw] "v"(vw),
           [vv] "v"((unsigned)O_VT + (c * 16 + 4 * w) * 1024 + lane * 16), [om0] "v"(om0), [om1] "v"(om1),
           [lrd] "v"(4096 + kg * 256 + i8 * 32 + up * 16), [idx] "v"(up ? 0xEEEEEEEEu : 0x44444444u),
           [oe] "v"(HO_EX + (4 * w + up) * 512 + kg * 128 + i8 * 16), [ost] "v"(HO_ST + hd * 64 + i8 * 8), [osp] "v"(HO_SP + ((4 * w + 2 * up) * 4 + kg) * 128 + i8 * 16),
           [sel0] "v"(onehot(up ? 8 : i8)), [sel1] "v"(onehot(up ? i8 : 8)), [mz] "v"(mz), [mw] "v"(mw),
           [lpw] "v"(w * 512 + n * 64 + kg * 16), [l8] "v"(lane * 8), [opub] "v"(w * HO_GRP + c * 256 + lane * 4),
           [opt] "v"(w * HO_GRP + HO_TS + c * 64 + lane * 4), [gm1] "v"(lane < 48 ? om1 : HO_TS + tq * 64), [lts] "v"(11264 + min(lane, 27) * 16), [lsw] "v"(2048 + w * 4), [lsr] "v"(2048)
        :
          "v0", "v1", "v2", "v3", "v4", "v5", "v6", "v7", "v8", "v9", "v10", "v11", "v12", "v13", "v14", "v15", "v16", "v17", "v18", "v19", "v20", "v21", "v22", "v23",
          "v24", "v25", "v26", "v27", "v28", "v29", "v30", "v31", "v32", "v33", "v34", "v35", "v36", "v37", "v38", "v39", "v40", "v41", "v42", "v43", "v44", "v45", "v46", "v47",
          "v48", "v49", "v50", "v51", "v52", "v53", "v54", "v55", "v56", "v57", "v58", "v59", "v60", "v61", "v62", "v63", "v64", "v65", "v66", "v67", "v68", "v69", "v70", "v71",
          "v72", "v73", "v74", "v75", "v76", "v77", "v78", "v79", "v80", "v81", "v82", "v83", "v84", "v85", "v86", "v87", "v88", "v89", "v90", "v91", "v92", "v93", "v94", "v95",
          "v96", "v97", "v98", "v99", "v100", "v101", "v102", "v103", "v104", "v105", "v106", "v107", "v108", "v109", "v110", "v111", "v112", "v113", "v114", "v115", "v116", "v117", "v118", "v119",
          "v120", "v121", "v122", "v123", "v124", "v125", "v126", "v127", "v128", "v129", "v130", "v131", "v132", "v133", "v134", "v135", "v136", "v137", "v138", "v139", "v140", "v141", "v142", "v143",
          "v144", "v145", "v146", "v147", "v148", "v149", "v150", "v151", "v152", "v153", "v154", "v155", "v156", "v157", "v158", "v159", "v160", "v161", "v162", "v163", "v164", "v165", "v166", "v167",
          "v168", "v169", "v170", "v171", "v172", "v173", "v174", "v175", "v176", "v177", "a0", "a1", "a2", "a3", "a4", "a5", "a6", "a7", "a8", "a9", "a10", "a11", "a12", "a13",
          "a14", "a15", "a16", "a17", "a18", "a19", "a20", "a21", "a22", "a23", "a24", "a25", "a26", "a27", "a28", "a29", "a30", "a31", "a32", "a33", "a34", "a35", "a36", "a37",
          "a38", "a39", "a40", "a41", "a42", "a43", "a44", "a45", "a46", "a47", "a48", "a49", "a50", "a51", "a52", "a53", "a54", "a55", "a56", "a57", "a58", "a59", "a60", "a61",
          "a62", "a63", "a64", "a65", "a66", "a67", "a68", "a69", "a70", "a71", "a72", "a73", "a74", "a75", "a76", "a77", "a78", "a79", "a80", "a81", "a82", "a83", "a84", "a85",
          "a86", "a87", "a88", "a89", "a90", "a91", "a92", "a93", "a94", "a95", "a96", "a97", "a98", "a99", "a100", "a101", "a102", "a103", "a104", "a105", "a106", "a107", "a108", "a109",
          "a110", "a111", "a112", "a113", "a114", "a115", "a116", "a117", "a118", "a119", "a120", "a121", "a122", "a123", "a124", "a125", "a126", "a127", "a128", "a129", "a130", "a131", "a132", "a133",
          "a134", "a135", "a136", "a137", "a138", "a139", "a140", "a141", "a142", "a143", "a144", "a145", "a146", "a147", "a148", "a149", "a150", "a151", "a152", "a153", "a154", "a155", "a156", "a157",
          "a158", "a159", "a160", "a161", "a162", "a163", "a164", "a165", "a166", "a167", "a168", "a169", "a170", "a171", "a172", "a173", "a174", "a175", "a176", "a177", "a178", "a179", "a180", "a181",
          "a182", "a183", "a184", "a185", "a186", "a187", "a188", "a189", "a190", "a191", "a192", "a193", "a194", "a195", "a196", "a197", "a198", "a199", "a200", "a201", "a202", "a203", "a204", "a205",
          "a206", "a207", "a208", "a209", "a210", "a211", "a212", "a213", "a214", "a215", "a216", "a217", "a218", "a219", "a220", "a221", "a222", "a223", "s40", "s41", "s42", "s43", "s44", "s45",
          "s46", "s47", "s48", "s49", "s50", "s51", "s52", "s53", "s54", "s55", "s56", "s57", "s58", "s59", "s60", "s61", "s62", "s63", "s64", "s65", "s66", "s67", "s68", "s69",
          "s70", "s71", "s72", "s73", "s74", "s75", "s76", "s77", "s78", "s79", "s80", "s81", "s82", "s83", "vcc", "scc", "memory");
    if (w < 2) DUMP(c * 2 + w);
}

DEV void u_worker(unsigned char* ws, rsrc_t wr, unsigned char* hob, int h, int w, int lane) {
    const int n = lane & 15, kg = lane >> 4;
    const rsrc_t hr = __builtin_amdgcn_make_buffer_rsrc(hob, 0, HO_BYTES, 0x00020000);
    const unsigned char* src = ws + O_WT + wtoff(7168 + h * 448 + 112 * w + n, kg);
    unsigned budget = SPIN_LIMIT;
    unsigned long long known = 0;
    {
        unsigned best[2] = {~0u, ~0u};
        for (int i = 0; i < NCAL; ++i) {
            unsigned long long t0, t1;
            asm volatile("s_memtime %0\n\ts_waitcnt lgkmcnt(0)" : "=s"(t0) :: "memory");
            bst1(hr, (h * 4 + w) * HO_STEP + HO_CAL + (i & 1) * 8192, 0, i);
            asm volatile("s_waitcnt vmcnt(0)\n\ts_memtime %0\n\ts_waitcnt lgkmcnt(0)" : "=s"(t1) :: "memory");
            const unsigned d = (unsigned)(t1 - t0);
            if (i & 1) best[1] = min(best[1], d); else best[0] = min(best[0], d);
        }
        if (lane == 0) bst1(wr, F_R + (h * 4 + w) * 4, (unsigned)O_FLG, best[1] < best[0] ? 2u : 1u);
        WAIT_VM(0);
        bar();
        FQ(64 + h, 0);
        if (w == 0 && lane == 0) arrive(ws, wr, F_S, 16);
        FQ(64 + h, 1);
    }
    u32x4 wf[98];
#pragma unroll
    for (int f = 0; f < UA; ++f) wf[f] = ldg(src + f * 1024);
#pragma unroll
    for (int f = 0; f < UA; ++f) asm volatile("" : "+a"(wf[f]));
#pragma unroll
    for (int f = UA; f < 98; ++f) wf[f] = ldg(src + f * 1024);
    all_wait(wr, F_S, budget, 1);
    const unsigned long long par = __ballot(bld1(wr, F_R + lane * 4, (unsigned)O_FLG) == 2u);
    const unsigned dst = (unsigned)O_UT + (kg >> 1) * 114688 + (7 * w + (kg & 1)) * 4096 + h * 256 + n * 16;
    for (int s = 0; s < NSEG; s += 2) {
        if (s < NSEG - 2) {
            if (w == 0) flag_wait(wr, F_P, max(s - 1, 0), known, budget, lane, 0);
            bar();
        }
        const int st = s + (n >> 3);
        const unsigned mo = st * HO_STEP + (((par >> st) & 1ull) ? 8192u : 0u) + (h & 3) * HO_GRP + memoff(n & 7, kg);
        u32x4 mf[14];
        for (;;) {
            fence_c();
            unsigned mx = 0;
#pragma unroll
            for (int q = 0; q < 14; ++q) mf[q] = bld4(hr, mo + q * 512, 0);
#pragma unroll
            for (int q = 0; q < 14; ++q) mx = max(mx, umax4(mf[q]));
            fence_c();
            if (!__any(mx == SENT) || budget == 0) break;
            --budget;
            __builtin_amdgcn_s_sleep(2);
        }
        FQ(64 + h, 2);
        unsigned r[8][2];
        WAIT_VM(0);
        bar();
        if (s && w == 3 && lane == 0) flag_set(wr, F_U + (s - 2) * 32 + h * 4);
        f32x4 a[7] = {};
#pragma unroll
        for (int q = 0; q < 14; ++q) {
#pragma unroll
            for (int t = 0; t < 7; ++t) a[t] = mfma16(mf[q], wf[t * 14 + q], a[t]);
            __builtin_amdgcn_sched_barrier(0);
        }
#pragma unroll
        for (int t = 0; t < 7; ++t) { r[t][0] = pk(a[t][0], a[t][1]); r[t][1] = pk(a[t][2], a[t][3]); }
        r[7][0] = r[6][0]; r[7][1] = r[6][1];
#pragma unroll
        for (int t = 0; t < 8; t += 2) {
            auto p0 = __builtin_amdgcn_permlane16_swap(r[t][0], r[t + 1][0], false, false);
            auto p1 = __builtin_amdgcn_permlane16_swap(r[t][1], r[t + 1][1], false, false);
            bst4(wr, dst + t * 4096 - (t == 6 ? (kg & 1) * 4096 : 0), s * 114688, u32x4{p0[0], p1[0], p0[1], p1[1]});
        }
#pragma unroll
        for (int f = 0; f < UA; ++f) asm volatile("" : "+a"(wf[f]));
    }
    WAIT_VM(0);
    bar();
    if (w == 0 && lane == 0) flag_set(wr, F_U + (NSEG - 2) * 32 + h * 4);
    FQ(64 + h, 3);
}

DEV void p_worker(rsrc_t wr, int xc, int w, int lane) {
    unsigned budget = SPIN_LIMIT;
    unsigned long long kv = 0;
    for (int s = 2 + w; s < NSEG - 1; s += 4) {
        if (s >= PFD) all_wait(wr, F_U + ((s - PFD) >> 1) * 64, budget, 1);
        flag_wait(wr, F_X + xc * 256, s, kv, budget, lane, 1);
        unsigned v[14];
#pragma unroll
        for (int i = 0; i < 14; ++i) v[i] = bld1(wr, (unsigned)O_VT + (xc + (i >> 1) * 4) * 16384 + (i & 1) * 8192 + lane * 128, s * 458752);
#pragma unroll
        for (int i = 0; i < 14; ++i) asm volatile("" :: "v"(v[i]));
    }
}

DEV void final_body(unsigned char* ws, rsrc_t wr, const u16* x, u16* out, int s, int g2, int w, int lane, unsigned& budget) {
    const int t = lane & 31, hf = lane >> 5;
    unsigned long long known = 0;
    const int pc = perm32(t), c0 = 7 * g2 + w, c1 = w < 3 ? 4 : 0;
    const unsigned vs = (unsigned)O_VT + (2 * c0 + (pc >> 4)) * 16384 + hf * 256 + (pc & 15) * 16;
    const unsigned us = (unsigned)O_UT + (2 * c0 + (pc >> 4)) * 4096 + hf * 256 + (pc & 15) * 16;
    u32x4 va[32], ua[8], ub[8], xv[4], gv[4];
    {
        u32x4 xf[28];
#pragma unroll
        for (int ks = 0; ks < 28; ++ks) xf[ks] = ldg(ws + O_XT + (size_t)s * 28672 + ks * 1024 + lane * 16);
#pragma unroll
        for (int ks = 0; ks < 28; ++ks) asm volatile("" : "+a"(xf[ks]));
        flag_wait(wr, F_K, s, known, budget, lane, 1);
        f32x16 sx[4];
        {
            const unsigned src = (unsigned)O_KT + 4 * w * 28672 + lane * 16;
#pragma unroll
            for (int g = 0; g < 4; ++g) {
                sx[g] = f32x16{};
#pragma unroll
                for (int k0 = 0; k0 < 28; k0 += 28 / FB) {
                    u32x4 ka[28 / FB];
#pragma unroll
                    for (int ks = 0; ks < 28 / FB; ++ks) ka[ks] = bld4(wr, src + g * 28672 + (k0 + ks) * 1024, s * 458752);
                    __builtin_amdgcn_sched_barrier(0);
#pragma unroll
                    for (int ks = 0; ks < 28 / FB; ++ks) sx[g] = mfma32(ka[ks], xf[k0 + ks], sx[g]);
                    __builtin_amdgcn_sched_barrier(0);
                }
            }
        }
        float mx[4], lx[4], ex[4][16];
#pragma unroll
        for (int g = 0; g < 4; ++g) {
            float m = -INFINITY, l = 0;
#pragma unroll
            for (int r = 0; r < 16; ++r) { const int key = 8 * (r >> 2) + 4 * hf + (r & 3); if (key > t) sx[g][r] = -INFINITY; m = fmaxf(m, sx[g][r]); }
            m = max32(m);
#pragma unroll
            for (int r = 0; r < 16; ++r) { ex[g][r] = __builtin_amdgcn_exp2f(sx[g][r] - m); l += ex[g][r]; asm volatile("" : "+v"(ex[g][r])); }
            mx[g] = m; lx[g] = l;
        }
        if (c1) {
#pragma unroll
            for (int k = 0; k < 32; ++k) __builtin_amdgcn_raw_ptr_buffer_load_lds(wr, (LAS void*)(uintptr_t)(40960 + w * 32768 + k * 1024), 16, vs + c1 * 32768 + k * 512, s * 458752, 0, SC1);
        }
        WAIT_VM(0);
#pragma unroll
        for (int k = 0; k < 32; ++k) va[k] = bld4(wr, vs + k * 512, s * 458752);
#pragma unroll
        for (int k = 0; k < 28; ++k) asm volatile("" : "+a"(va[k]));
        known = 0; flag_wait(wr, F_G, s, known, budget, lane, 1);
        all_wait(wr, s < NSEG - 4 ? F_U + ((s >> 1) + 1) * 64 : s < NSEG - 2 ? F_P + s * 4 : F_D + w * 128 + (lane & 15) * 4, budget, s < NSEG - 2);
        FQ(s, 0);
        f32x16 sm = {};
        {
            const unsigned src = (unsigned)O_RT + w * 28672 + lane * 16;
            u32x4 ra[28];
#pragma unroll
            for (int ks = 0; ks < 28; ++ks) ra[ks] = bld4(wr, src + ks * 1024, s * 114688);
            __builtin_amdgcn_sched_barrier(0);
#pragma unroll
            for (int ks = 0; ks < 28; ++ks) sm = mfma32(ra[ks], xf[ks], sm);
        }
        __builtin_amdgcn_sched_barrier(0);
        fence_c();
        const unsigned uf = bld1(wr, F_U + (s >> 1) * 64 + (lane & 15) * 4, (unsigned)O_FLG);
        fence_c();
#pragma unroll
        for (int i = 0; i < 4; ++i) {
            const int cc = c0 + (i >> 1) * c1;
            xv[i] = ldg(x + (size_t)(s * 32 + t) * 448 + 32 * cc + 16 * (i & 1) + 8 * hf);
            gv[i] = bld4(wr, (unsigned)O_GT + (cc * 2 + (i & 1)) * 1024 + lane * 16, s * 28672);
        }
        __builtin_amdgcn_sched_barrier(0);
        FQ(s, 2);
        unsigned pm[4][2];
#pragma unroll
        for (int g = 0; g < 4; ++g) {
            const float m = fmaxf(mx[g], max32(fmaxf(fmaxf(sm[4 * g], sm[4 * g + 1]), fmaxf(sm[4 * g + 2], sm[4 * g + 3]))));
            const float fx = __builtin_amdgcn_exp2f(mx[g] - m);
            float l = lx[g] * fx, em[4];
#pragma unroll
            for (int r = 0; r < 4; ++r) { em[r] = __builtin_amdgcn_exp2f(sm[4 * g + r] - m); l += em[r]; }
            const float inv = 1.0f / sum32(l), fi = fx * inv;
#pragma unroll
            for (int r = 0; r < 16; ++r) sx[g][r] = ex[g][r] * fi;
            sts128(((4 * w + g) * 2) * 1024 + lane * 16, pk8(sx[g], 0));
            sts128(((4 * w + g) * 2 + 1) * 1024 + lane * 16, pk8(sx[g], 8));
            pm[g][0] = pk(em[0] * inv, em[1] * inv); pm[g][1] = pk(em[2] * inv, em[3] * inv);
        }
#pragma unroll
        for (int g = 0; g < 4; g += 2) {
            auto p0 = __builtin_amdgcn_permlane32_swap(pm[g][0], pm[g + 1][0], false, false);
            auto p1 = __builtin_amdgcn_permlane32_swap(pm[g][1], pm[g + 1][1], false, false);
            sts128(32768 + (2 * w + (g >> 1)) * 1024 + lane * 16, u32x4{p0[0], p1[0], p0[1], p1[1]});
        }
        fence_c();
        if (!__all(uf != 0u)) all_wait(wr, F_U + (s >> 1) * 64 + (lane & 15) * 4, budget, 0);
        fence_c();
        FQ(s, 1);
#pragma unroll
        for (int k = 0; k < 8; ++k) ua[k] = bld4(wr, us + k * 512, s * 114688);
#pragma unroll
        for (int k = 0; k < 8; ++k) ub[k] = bld4(wr, us + c1 * 8192 + k * 512, s * 114688);
    }
    WAIT_LGKM0();
    bar();
    FQ(s, 3);
    __builtin_amdgcn_sched_barrier(0);
    const LAS u32x4* pb = (const LAS u32x4*)(uintptr_t)(lane * 16);
    const LAS u32x4* vb = pb + (40 + min(w, 2) * 32) * 64;
    f32x16 acc[2] = {};
    u32x4 pf[4], qf[4];
#pragma unroll
    for (int k = 0; k < 4; ++k) { pf[k] = pb[k * 64]; qf[k] = vb[k * 64]; }
#pragma unroll
    for (int k = 0; k < 40; ++k) {
        acc[0] = mfma32(k < 32 ? va[k] : ua[k - 32], pf[k & 3], acc[0]);
        acc[1] = mfma32(k < 32 ? qf[k & 3] : ub[k - 32], pf[k & 3], acc[1]);
        if (k + 4 < 40) pf[k & 3] = pb[(k + 4) * 64];
        if (k + 4 < 32) qf[k & 3] = vb[(k + 4) * 64];
        __builtin_amdgcn_sched_barrier(0);
    }
#pragma unroll
    for (int i = 0; i < 2; ++i) {
        if (i && !c1) break;
        u16* o = out + (size_t)(s * 32 + t) * 448 + 32 * (c0 + i * c1) + 8 * hf;
#pragma unroll
        for (int q = 0; q < 8; ++q) { const int r = 2 * i + (q >> 2); acc[i][2 * q] += blo(xv[r][q & 3]) + blo(gv[r][q & 3]); acc[i][2 * q + 1] += bhi(xv[r][q & 3]) + bhi(gv[r][q & 3]); }
        stg(o, pk8(acc[i], 0));
        stg(o + 16, pk8(acc[i], 8));
        FQ(s, 4 + i);
    }
}

__global__ void __launch_bounds__(256, 1) k_main(unsigned char* ws, unsigned char* hob, const u16* __restrict__ x, const u16* __restrict__ Wg, const u16* __restrict__ Wu,
                                                 const u16* __restrict__ Wd, u16* __restrict__ out) {
    const int b = blockIdx.x, lane = threadIdx.x & 63, w = __builtin_amdgcn_readfirstlane(threadIdx.x >> 6);
    const int xv = (b + (int)((const unsigned*)(ws + O_PAR))[64]) & 7;
    const int id = (b >> 3) * 4 + (xv & 3);
    const rsrc_t wr = __builtin_amdgcn_make_buffer_rsrc(ws, 0, WS_SPAN, 0x00020000);
    if (xv < 4) {
        const u32x4 rh = {(unsigned)(uintptr_t)hob, (unsigned)((uintptr_t)hob >> 32), HO_BYTES, 0x00020000u};
        const u32x4 rw = {(unsigned)(uintptr_t)ws, (unsigned)((uintptr_t)ws >> 32), WS_SPAN, 0x00020000u};
        const unsigned t0 = __builtin_amdgcn_readfirstlane(((const unsigned*)(ws + O_PAR))[65]);
        if (id < 28) c_worker(ws, wr, rw, rh, t0, id, w, lane);
        else if (id < 92) a_worker(ws, wr, rw, rh, t0, (id - 28) >> 2, (id - 28) & 3, w, lane);
        else if (id < 108) x_worker(ws, rw, rh, t0, id - 92, w, lane);
        else if (id < 112) p_worker(wr, id & 3, w, lane);
        else u_worker(ws, wr, hob, id - 112, w, lane);
        return;
    }
    unsigned budget = SPIN_LIMIT;
#define SQ(i)
    SQ(0);
    if (id >= 72) { gemm_body<false>(ws, wr, (id - 72) * 4 + w, w, lane); SQ(1);
    }
    else if (id < 70) {
        if (id < 56) gemm_body<true>(ws, wr, id * 4 + w, w, lane);
        all_wait(wr, F_K, budget, 1);
        gluh_body(ws, wr, Wg, Wu, (id % 14) * 4 + w, (id / 14) * 13, min((id / 14) * 13 + 13, NSEG), w, lane);
        SQ(1);
        glu2_body(ws, wr, Wd, id % 7, (id / 7) * 64 / 10, (id / 7 + 1) * 64 / 10, w, lane, budget);
        SQ(2);
    }
    if (id == 71 && w == 0) {
        const unsigned fo = F_K;
        bool done = false;
        unsigned early = 0;
        while (budget) {
            fence_c();
            bool now = bld1(wr, lane * 4, (unsigned)O_FLG + F_CNT + fo) == 56u;
            if (early < 6) {
                const u32x2 z = bld2(wr, F_Z + min(lane, 55) * 8, (unsigned)O_FLG);
                while (early < 6 && __all(min(z.x, z.y) >= early + 2)) early += 2;
            }
            now = now || lane < early;
            if (now && !done) {
#pragma unroll
                for (int i = 0; i < 4; ++i) flag_set(wr, F_X + i * 256 + lane * 4);
                flag_set(wr, fo + lane * 4);
            }
            done = now;
            if (__all(done)) break;
            --budget;
            __builtin_amdgcn_s_sleep(MS);
        }
    }
    const int fs = (id >> 3) * 4 + (id & 3);
    all_wait(wr, F_U + (PA + (fs * PB >> 4)) * 64, budget, 1);
    for (int i = (fs * PB) & 15; i > 0; --i) __builtin_amdgcn_s_sleep(PC);
    SQ(3);
    final_body(ws, wr, x, out, fs, (id >> 2) & 1, w, lane, budget);
}

extern "C" void kernel_launch(void* const* d_in, const int* in_sizes, int n_in, void* d_out, int out_size, void* d_ws, size_t ws_size, hipStream_t stream) {
    const u16* x = (const u16*)d_in[0];
    unsigned char* ws = (unsigned char*)d_ws;
    unsigned char* hob = (unsigned char*)(((uintptr_t)ws + O_HO + 0x1FFFFF) & ~(uintptr_t)0x1FFFFF);
    (void)hipFuncSetAttribute((const void*)k_prep, hipFuncAttributeMaxDynamicSharedMemorySize, 147456);
    (void)hipFuncSetAttribute((const void*)k_main, hipFuncAttributeMaxDynamicSharedMemorySize, 139264);
    k_prep<<<256, 256, 147456, stream>>>(x, (const u16*)d_in[1], (const u16*)d_in[2], (const u16*)d_in[3], (const u16*)d_in[4], (const u16*)d_in[5], ws, hob);
    k_main<<<256, 256, 139264, stream>>>(ws, hob, x, (const u16*)d_in[8], (const u16*)d_in[9], (const u16*)d_in[10], (u16*)d_out);
}
```
